```python
import torch

def _softmax(x, axis=-1):
    return torch.softmax(x, dim=axis)
def _gelu(x):
    return torch.nn.functional.gelu(x, approximate="tanh")
def _silu(x):
    return torch.nn.functional.silu(x)
def _sigmoid(x):
    return torch.sigmoid(x)
def _layernorm(x, axis=-1, eps=1e-5):
    m = x.mean(dim=axis, keepdim=True)
    v = x.var(dim=axis, keepdim=True, unbiased=False)
    return (x - m) / torch.sqrt(v + eps)
def _scatter_add(tgt, idx, src, axis=0):
    return tgt.clone().index_add_(axis, idx, src)
def _segment_sum(src, seg, n):
    out = torch.zeros((n,) + src.shape[1:], dtype=src.dtype)
    return out.index_add_(0, seg, src)
def _pad(x, pw):
    flat = tuple(v for pair in reversed(pw) for v in pair)
    return torch.nn.functional.pad(x, flat)

kernel_name = 'standalone__gqa_softmax__near_ridge'

def setup_inputs():

    _w = (lambda a: (lambda t: t.to(torch.bfloat16) if t.is_floating_point() else t)(torch.as_tensor(a)))
    B, Hq, Hkv, Sq, Sk, D = 8, 32, 32, 256, 2048, 128
    return {
        "q": _w((lambda *s: torch.randn(*s, dtype=torch.bfloat16) * 0.8)(B, Hq, Sq, D)),
        "k": _w((lambda *s: torch.randn(*s, dtype=torch.bfloat16) * 0.8)(B, Hkv, Sk, D)),
        "v": _w((lambda *s: torch.randn(*s, dtype=torch.bfloat16) * 0.05)(B, Hkv, Sk, D)),
    }

def reference(q, k, v):
    B, Hq, Sq, D = q.shape
    Hkv = k.shape[1]
    rep = Hq // Hkv
    k = (lambda x, n, axis: torch.repeat_interleave(x, n, dim=axis))(k, rep, axis=1)
    v = (lambda x, n, axis: torch.repeat_interleave(x, n, dim=axis))(v, rep, axis=1)
    scale = D ** -0.5
    scores = torch.einsum("bhqd,bhkd->bhqk", q, k) * scale
    p = _softmax(scores, axis=-1)
    return torch.einsum("bhqk,bhkd->bhqd", p, v)
```

```cpp
#include <hip/hip_runtime.h>
#include <hip/hip_bf16.h>
#include <cstdio>
using bf16 = __hip_bfloat16;
constexpr int   D = 128, NW = 8, QBLK = 32, KVBLK = 64;
constexpr float SCALE = 0.088388347648318440f;
constexpr float THR = 8.f;
constexpr int SDEPTH = 2;
constexpr int B = 8, HQ = 32, HKV = 32, S = 256, SKV = 2048;
constexpr int LDQ = D, LDK = D, LDO = D;
constexpr size_t SHM_V = KVBLK * D * 2, SHM_K = KVBLK * D * 2, SHM_ATTN = 2 * SHM_V + 2 * SHM_K + NW * 64 * 4;
using bf16x8 = __attribute__((ext_vector_type(8))) short;
using s16x4  = __attribute__((ext_vector_type(4))) short;
using f32x16 = __attribute__((ext_vector_type(16))) float;
using f32x8  = __attribute__((ext_vector_type(8))) float;
using u32x4  = __attribute__((ext_vector_type(4))) unsigned;
#define KSWZ(row, colB) ((row) * 256 + ((colB) ^ (((row) & 7) << 4)))
#define SBAR() __builtin_amdgcn_sched_barrier(0)
__device__ __forceinline__ int crow(int r, int hi) { return (r & 3) + 8 * (r >> 2) + 4 * hi; }
__device__ __forceinline__ unsigned cvtpk(float lo, float hi) {
  unsigned r; asm volatile("v_cvt_pk_bf16_f32 %0, %1, %2" : "=v"(r) : "v"(lo), "v"(hi)); return r;
}
__device__ static __forceinline__ bf16x8 ld8(const bf16* p) { return *reinterpret_cast<const bf16x8*>(p); }
__device__ __forceinline__ void partialSM(f32x16& p0, f32x16& p1, float& m_reg, float& mn, float& alpha) {
  constexpr float C = SCALE * 1.4426950408889634f;
  float pmax = p0[0]; for (int r = 1; r < 16; ++r) pmax = fmaxf(pmax, p0[r]); for (int r = 0; r < 16; ++r) pmax = fmaxf(pmax, p1[r]);
  { auto rr = __builtin_amdgcn_permlane32_swap(__float_as_uint(pmax), __float_as_uint(pmax), false, false);
    pmax = fmaxf(__uint_as_float(rr[0]), __uint_as_float(rr[1])); }
  if (__builtin_expect(__all(pmax - m_reg <= THR / SCALE), 1)) { mn = m_reg; alpha = 1.f; }
  else { mn = fmaxf(m_reg, pmax); alpha = __builtin_amdgcn_exp2f((m_reg - mn) * C); m_reg = mn; }
  float mnC = -mn * C;
  for (int r = 0; r < 16; ++r) p0[r] = fmaf(p0[r], C, mnC); for (int r = 0; r < 16; ++r) p1[r] = fmaf(p1[r], C, mnC);
  for (int r = 0; r < 16; ++r) p0[r] = __builtin_amdgcn_exp2f(p0[r]);
}
__device__ __forceinline__ void finishSM(f32x16& p0, f32x16& p1, float alpha, float& l_reg, bf16x8& pa0, bf16x8& pa1, bf16x8& pa2, bf16x8& pa3) {
  for (int r = 0; r < 16; ++r) p1[r] = __builtin_amdgcn_exp2f(p1[r]);
  float ps = 0; for (int r = 0; r < 16; ++r) ps += p0[r]; for (int r = 0; r < 16; ++r) ps += p1[r];
  { auto rr = __builtin_amdgcn_permlane32_swap(__float_as_uint(ps), __float_as_uint(ps), false, false);
    ps = __uint_as_float(rr[0]) + __uint_as_float(rr[1]); }
  l_reg = l_reg * alpha + ps;
#define PK4(P, BASE, OUT) do { unsigned a0 = cvtpk(P[BASE + 0], P[BASE + 1]), a1 = cvtpk(P[BASE + 2], P[BASE + 3]);   \
    unsigned b0 = cvtpk(P[BASE + 4], P[BASE + 5]), b1 = cvtpk(P[BASE + 6], P[BASE + 7]);                              \
    auto r0 = __builtin_amdgcn_permlane32_swap(a0, b0, false, false); auto r1 = __builtin_amdgcn_permlane32_swap(a1, b1, false, false); \
    u32x4 w = {r0[0], r1[0], r0[1], r1[1]}; OUT = *reinterpret_cast<bf16x8*>(&w); } while (0)
  PK4(p0, 0, pa0); PK4(p0, 8, pa1); PK4(p1, 0, pa2); PK4(p1, 8, pa3);
#undef PK4
}
__device__ __forceinline__ void qkt(f32x16& p0, f32x16& p1, const bf16* Ks, const bf16x8* qr, int r32, int hi) {
  p0 = f32x16{}; p1 = f32x16{};
  for (int d0 = 0; d0 < 8; ++d0) { int cb = (d0 * 16 + hi * 8) * 2;
    bf16x8 b0 = *reinterpret_cast<const bf16x8*>((const char*)Ks + KSWZ(r32, cb));
    bf16x8 b1 = *reinterpret_cast<const bf16x8*>((const char*)Ks + KSWZ(32 + r32, cb));
    p0 = __builtin_amdgcn_mfma_f32_32x32x16_bf16(b0, qr[d0], p0, 0, 0, 0);
    p1 = __builtin_amdgcn_mfma_f32_32x32x16_bf16(b1, qr[d0], p1, 0, 0, 0); }
}
__device__ __forceinline__ int v_st(int k, int c) { const int kk = (k & ~0xC) | ((k & 4) << 1) | ((k & 8) >> 1); return ((kk >> 3) * 4 + (c >> 5)) * 512 + ((kk & 7) * 32 + (c & 31)) * 2; }
__device__ __forceinline__ int v_rd_base(int lane) { return ((lane & 3) << 3) | (((lane >> 2) & 3) << 6) | (((lane >> 4) & 1) << 5) | (((lane >> 5) & 1) << 8); }
constexpr int v_rd_off(int d0, int ks, int half) { return d0 * 512 + ks * 4096 + half * 2048; }
template <int OFF> __device__ __forceinline__ s16x4 tr_read(int vb) {
  s16x4 r; asm volatile("ds_read_b64_tr_b16 %0, %1 offset:%2" : "=&v"(r) : "v"(vb), "i"(OFF) : "memory"); return r;
}
template <int D0> __device__ __forceinline__ void pv_one(f32x16& od, int vb, bf16x8 pa0, bf16x8 pa1, bf16x8 pa2, bf16x8 pa3) {
  const s16x4 l0 = tr_read<v_rd_off(D0, 0, 0)>(vb), h0 = tr_read<v_rd_off(D0, 0, 1)>(vb), l1 = tr_read<v_rd_off(D0, 1, 0)>(vb), h1 = tr_read<v_rd_off(D0, 1, 1)>(vb);
  const s16x4 l2 = tr_read<v_rd_off(D0, 2, 0)>(vb), h2 = tr_read<v_rd_off(D0, 2, 1)>(vb), l3 = tr_read<v_rd_off(D0, 3, 0)>(vb), h3 = tr_read<v_rd_off(D0, 3, 1)>(vb);
  asm volatile("s_waitcnt lgkmcnt(0)" ::: "memory"); SBAR();
#define PK(L, H) (bf16x8){L[0], L[1], L[2], L[3], H[0], H[1], H[2], H[3]}
  od = __builtin_amdgcn_mfma_f32_32x32x16_bf16(pa0, PK(l0, h0), od, 0, 0, 0);
  od = __builtin_amdgcn_mfma_f32_32x32x16_bf16(pa1, PK(l1, h1), od, 0, 0, 0);
  od = __builtin_amdgcn_mfma_f32_32x32x16_bf16(pa2, PK(l2, h2), od, 0, 0, 0);
  od = __builtin_amdgcn_mfma_f32_32x32x16_bf16(pa3, PK(l3, h3), od, 0, 0, 0);
#undef PK
}
__device__ __forceinline__ void pv_d0(f32x16* o, int vb, bf16x8 pa0, bf16x8 pa1, bf16x8 pa2, bf16x8 pa3) {
  pv_one<0>(o[0], vb, pa0, pa1, pa2, pa3); pv_one<1>(o[1], vb, pa0, pa1, pa2, pa3); pv_one<2>(o[2], vb, pa0, pa1, pa2, pa3); pv_one<3>(o[3], vb, pa0, pa1, pa2, pa3);
}
__device__ __forceinline__ void attn_dense_body(const bf16* __restrict__ Qb, const bf16* __restrict__ Kh, const bf16* __restrict__ Vh,
                                                bf16* __restrict__ Ob, int seq, char* lds) {
  const int tid = threadIdx.x, wid = tid >> 6, lane = tid & 63, r32 = lane & 31, hi = lane >> 5;
  bf16* V_lds = (bf16*)lds; bf16* K_lds = (bf16*)(lds + 2 * SHM_V);
  float* ws = (float*)(lds + 2 * SHM_V + 2 * SHM_K) + wid * 64; float* li_l = ws; float* al_l = ws + 32;
  float m_reg = -1e30f, l_reg = 0; f32x16 o[4] = {}; bf16x8 qr[8];
  const bf16* Qw = Qb + (long)(wid * QBLK + r32) * LDQ + hi * 8;
#pragma unroll
  for (int d0 = 0; d0 < 8; ++d0) qr[d0] = ld8(Qw + d0 * 16);
  const int sr = tid >> 4, sc = (tid & 15) * 8, vst0 = v_st(sr, sc), vst1 = v_st(32 + sr, sc);
  const int vb0 = (int)(uintptr_t)V_lds + v_rd_base(lane);
  struct { bf16x8 vs0, vs1, ks0, ks1; } sr_[SDEPTH];
#define SLOAD(i, k0) do { sr_[i].vs0 = ld8(&Vh[(long)((k0) + sr) * LDK + sc]); sr_[i].vs1 = ld8(&Vh[(long)((k0) + 32 + sr) * LDK + sc]); \
    sr_[i].ks0 = ld8(&Kh[(long)((k0) + sr) * LDK + sc]); sr_[i].ks1 = ld8(&Kh[(long)((k0) + 32 + sr) * LDK + sc]); } while (0)
#define SWRITE(b, i) do { *(bf16x8*)((char*)V_lds + (b) * SHM_V + vst0) = sr_[i].vs0;          \
    *(bf16x8*)((char*)V_lds + (b) * SHM_V + vst1) = sr_[i].vs1; int kc = sc * 2;               \
    *(bf16x8*)((char*)K_lds + (b) * SHM_K + KSWZ(sr, kc)) = sr_[i].ks0;                       \
    *(bf16x8*)((char*)K_lds + (b) * SHM_K + KSWZ(32 + sr, kc)) = sr_[i].ks1; } while (0)
#define SWAIT() do { asm volatile("s_waitcnt vmcnt(4)" ::: "memory"); } while (0)
#define RESC(a) do { if (__any((a) < 1.f)) { if (hi == 0) al_l[r32] = (a); asm volatile("s_waitcnt lgkmcnt(0)" ::: "memory"); \
    for (int d = 0; d < 4; ++d) for (int r = 0; r < 16; ++r) o[d][r] *= al_l[crow(r, hi)]; } } while (0)
  f32x16 pA0, pA1, pB0, pB1; float mnA, mnB, alA, alB; bf16x8 pa0, pa1, pa2, pa3; const int NT = seq / KVBLK;
  constexpr int SE = 0, SO = SDEPTH - 1;
  SLOAD(SE, 0); asm volatile("s_waitcnt vmcnt(0)" ::: "memory"); SWRITE(0, SE); __syncthreads();
  qkt(pA0, pA1, K_lds, qr, r32, hi); partialSM(pA0, pA1, m_reg, mnA, alA);
  SLOAD(SO, KVBLK); if (2 < NT) SLOAD(SE, 2 * KVBLK);
  SWAIT(); SWRITE(1, SO); __syncthreads();
  for (int j = 1; j + 1 < NT; j += 2) {
    SBAR(); qkt(pB0, pB1, (bf16*)((char*)K_lds + SHM_K), qr, r32, hi);
    finishSM(pA0, pA1, alA, l_reg, pa0, pa1, pa2, pa3); SBAR();
    SLOAD(SO, (j + SDEPTH) * KVBLK); SBAR();
    pv_d0(o, vb0, pa0, pa1, pa2, pa3); partialSM(pB0, pB1, m_reg, mnB, alB);
    __syncthreads(); SWAIT(); SWRITE(0, SE);
    RESC(alB); __syncthreads();
    SBAR(); qkt(pA0, pA1, K_lds, qr, r32, hi);
    finishSM(pB0, pB1, alB, l_reg, pa0, pa1, pa2, pa3); SBAR();
    if (j + 3 < NT) SLOAD(SE, (j + 1 + SDEPTH) * KVBLK); SBAR();
    pv_d0(o, vb0 + (int)SHM_V, pa0, pa1, pa2, pa3); partialSM(pA0, pA1, m_reg, mnA, alA);
    __syncthreads(); SWAIT(); SWRITE(1, SO);
    RESC(alA); __syncthreads();
  }
  SBAR(); qkt(pB0, pB1, (bf16*)((char*)K_lds + SHM_K), qr, r32, hi);
  finishSM(pA0, pA1, alA, l_reg, pa0, pa1, pa2, pa3); SBAR();
  pv_d0(o, vb0, pa0, pa1, pa2, pa3); partialSM(pB0, pB1, m_reg, mnB, alB);
  __syncthreads(); RESC(alB);
  finishSM(pB0, pB1, alB, l_reg, pa0, pa1, pa2, pa3); SBAR();
  pv_d0(o, vb0 + (int)SHM_V, pa0, pa1, pa2, pa3);
  if (hi == 0) li_l[r32] = l_reg; asm volatile("s_waitcnt lgkmcnt(0)" ::: "memory");
  float rli[16];
#pragma unroll
  for (int r = 0; r < 16; ++r) rli[r] = __builtin_amdgcn_rcpf(li_l[crow(r, hi)]);
  bf16* Ow = Ob + (long)(wid * QBLK) * LDO;
#pragma unroll
  for (int r = 0; r < 16; ++r) { int orow = crow(r, hi);
    for (int d0 = 0; d0 < 4; ++d0) Ow[(long)orow * LDO + d0 * 32 + r32] = __float2bfloat16(o[d0][r] * rli[r]); }
}
__global__ __launch_bounds__(64 * NW, 1)
void attn_fwd_dense(const bf16* __restrict__ Q, const bf16* __restrict__ K, const bf16* __restrict__ V, bf16* __restrict__ O) {
  extern __shared__ __attribute__((aligned(16))) char shm[];
  const long bh = blockIdx.x;
  attn_dense_body(Q + bh * S * D, K + bh * SKV * D, V + bh * SKV * D, O + bh * S * D, SKV, shm);
}
extern "C" void kernel_launch(void* const* d_in, const int* in_sizes, int n_in, void* d_out, int out_size, void* d_ws, size_t ws_size, hipStream_t stream) {
  (void)hipFuncSetAttribute((const void*)attn_fwd_dense, hipFuncAttributeMaxDynamicSharedMemorySize, (int)SHM_ATTN);
  hipLaunchKernelGGL(attn_fwd_dense, dim3(B * HQ), dim3(64 * NW), SHM_ATTN, stream,
                     (const bf16*)d_in[0], (const bf16*)d_in[1], (const bf16*)d_in[2], (bf16*)d_out);
}
```

```cpp
#include <hip/hip_runtime.h>
#include <hip/hip_bf16.h>

typedef __bf16 bf16x8_t __attribute__((ext_vector_type(8)));
typedef __bf16 bf16x2_t __attribute__((ext_vector_type(2)));
typedef float f32x4_t __attribute__((ext_vector_type(4)));
typedef float f32x2_t __attribute__((ext_vector_type(2)));
typedef unsigned u32x4_t __attribute__((ext_vector_type(4)));
typedef unsigned u32x2_t __attribute__((ext_vector_type(2)));

__device__ __forceinline__ unsigned pack_bf16(float lo, float hi) {
  f32x2_t v = {lo, hi};
  return __builtin_bit_cast(unsigned, __builtin_convertvector(v, bf16x2_t));
}

__device__ __forceinline__ void exact_rows(const char* qp, const char* kp, const char* vp, char* op, unsigned w) {
  const unsigned lane = __builtin_amdgcn_mbcnt_hi(~0u, __builtin_amdgcn_mbcnt_lo(~0u, 0u));
  const unsigned r = lane & 15u, g = lane >> 4;
  const float c = 0.12751743082f;
  for (unsigned qb = 0; qb < 4; ++qb) {
    const unsigned row = 64u * w + 16u * qb + r;
    bf16x8_t qf[4];
#pragma unroll
    for (int dd = 0; dd < 4; ++dd) {
      const u32x4_t x = *(const u32x4_t*)(qp + row * 256u + 64u * dd + 16u * g);
      u32x4_t y;
#pragma unroll
      for (int i = 0; i < 4; ++i) y[i] = pack_bf16(__uint_as_float(x[i] << 16) * c, __uint_as_float(x[i] & 0xffff0000u) * c);
      qf[dd] = __builtin_bit_cast(bf16x8_t, y);
    }
    float m = -__builtin_inff();
    for (unsigned kb = 0; kb < 128; ++kb) {
      f32x4_t s = {0.f, 0.f, 0.f, 0.f};
#pragma unroll
      for (int dd = 0; dd < 4; ++dd)
        s = __builtin_amdgcn_mfma_f32_16x16x32_bf16(*(const bf16x8_t*)(kp + (16u * kb + r) * 256u + 64u * dd + 16u * g), qf[dd], s, 0, 0, 0);
      m = fmaxf(fmaxf(fmaxf(m, s[0]), fmaxf(s[1], s[2])), s[3]);
      if (s[0] != s[0] || s[1] != s[1] || s[2] != s[2] || s[3] != s[3]) m = __builtin_nanf("");
    }
    {
      float o = __shfl_xor(m, 16); m = (m != m || o != o) ? __builtin_nanf("") : fmaxf(m, o);
      o = __shfl_xor(m, 32); m = (m != m || o != o) ? __builtin_nanf("") : fmaxf(m, o);
    }
    f32x4_t acc[8];
#pragma unroll
    for (int db = 0; db < 8; ++db) acc[db] = f32x4_t{0.f, 0.f, 0.f, 0.f};
    float l = 0.f;
    for (unsigned u = 0; u < 64; ++u) {
      u32x4_t pw;
#pragma unroll
      for (int kb = 0; kb < 2; ++kb) {
        f32x4_t s = {0.f, 0.f, 0.f, 0.f};
#pragma unroll
        for (int dd = 0; dd < 4; ++dd)
          s = __builtin_amdgcn_mfma_f32_16x16x32_bf16(*(const bf16x8_t*)(kp + (32u * u + 16u * kb + r) * 256u + 64u * dd + 16u * g), qf[dd], s, 0, 0, 0);
        const float p0 = __builtin_amdgcn_exp2f(s[0] - m), p1 = __builtin_amdgcn_exp2f(s[1] - m), p2 = __builtin_amdgcn_exp2f(s[2] - m), p3 = __builtin_amdgcn_exp2f(s[3] - m);
        pw[2 * kb] = pack_bf16(p0, p1); pw[2 * kb + 1] = pack_bf16(p2, p3);
        l += __uint_as_float(pw[2 * kb] << 16) + __uint_as_float(pw[2 * kb] & 0xffff0000u) + __uint_as_float(pw[2 * kb + 1] << 16) + __uint_as_float(pw[2 * kb + 1] & 0xffff0000u);
      }
      const bf16x8_t pf = __builtin_bit_cast(bf16x8_t, pw);
#pragma unroll
      for (int db = 0; db < 8; ++db) {
        u32x4_t vw;
#pragma unroll
        for (int j2 = 0; j2 < 4; ++j2) {
          const unsigned key = 32u * u + 16u * (j2 >> 1) + 4u * g + 2u * (j2 & 1);
          const unsigned short* vk = (const unsigned short*)(vp + key * 256u) + 16u * db + r;
          vw[j2] = (unsigned)vk[0] | ((unsigned)vk[128] << 16);
        }
        acc[db] = __builtin_amdgcn_mfma_f32_16x16x32_bf16(__builtin_bit_cast(bf16x8_t, vw), pf, acc[db], 0, 0, 0);
      }
    }
    l += __shfl_xor(l, 16);
    l += __shfl_xor(l, 32);
    const float il = 1.f / l;
#pragma unroll
    for (int db = 0; db < 8; ++db) {
      u32x2_t o = {pack_bf16(acc[db][0] * il, acc[db][1] * il), pack_bf16(acc[db][2] * il, acc[db][3] * il)};
      *(u32x2_t*)(op + row * 256u + 32u * db + 8u * g) = o;
    }
  }
}

__global__ __launch_bounds__(256, 1) void standalone__gqa_softmax__near_ridge_kernel(const char* __restrict__ Q, const char* __restrict__ K, const char* __restrict__ V, char* __restrict__ O
) {
  extern __shared__ __attribute__((aligned(16))) char lds[];
  const unsigned bh = blockIdx.x;
  const unsigned w = __builtin_amdgcn_readfirstlane(threadIdx.x >> 6);
  const char* qp = Q + (size_t)bh * 65536;
  const char* kp = K + (size_t)bh * 524288;
  const char* vp = V + (size_t)bh * 524288;
  char* op = O + (size_t)bh * 65536;
  const unsigned ldsb = __builtin_amdgcn_readfirstlane((unsigned)(size_t)lds);
  unsigned flag;
  asm volatile(
    "Lstart_%=:\n"
    "s_getpc_b64 s[92:93]\n"
    "s_memtime s[82:83]\n"
    "s_memrealtime s[84:85]\n"
    "v_mbcnt_lo_u32_b32 v242, -1, 0\n"
    "v_mbcnt_hi_u32_b32 v242, -1, v242\n"
    "s_mov_b64 s[40:41], %[k]\n"
    "s_and_b32 s41, s41, 0xffff\n"
    "s_mov_b32 s42, 524288\n"
    "s_mov_b32 s43, 0x20000\n"
    "s_lshl_b32 s59, %[w], 12\n"
    "s_add_u32 s57, s59, %[lds]\n"
    "v_lshrrev_b32 v244, 4, v242\n"
    "v_and_b32 v245, 15, v242\n"
    "v_xor_b32 v246, v245, v244\n"
    "v_lshlrev_b32 v246, 4, v246\n"
    "v_lshl_add_u32 v236, v244, 8, v246\n"
    "s_mov_b32 m0, s57\n"
    "v_xor_b32 v237, 64, v236\n"
    "v_xor_b32 v238, 128, v236\n"
    "v_xor_b32 v239, 192, v236\n"
    "buffer_load_dwordx4 v236, s[40:43], s59 offen nt lds\n"
    "buffer_load_dwordx4 v237, s[40:43], s59 offen offset:1024 nt lds\n"
    "buffer_load_dwordx4 v238, s[40:43], s59 offen offset:2048 nt lds\n"
    "buffer_load_dwordx4 v239, s[40:43], s59 offen offset:3072 nt lds\n"
    "s_mov_b64 s[48:49], %[q]\n"
    "s_and_b32 s49, s49, 0xffff\n"
    "s_mov_b32 s50, 65536\n"
    "s_mov_b32 s51, 0x20000\n"
    "s_lshl_b32 s56, %[w], 14\n"
    "s_add_u32 s63, s56, 98304\n"
    "s_cmp_eq_u32 %[w], 0\n"
    "s_cselect_b32 s63, 49152, s63\n"
    "s_add_u32 s64, s63, %[lds]\n"
    "s_add_u32 m0, s64, 0\n"
    "s_add_u32 s65, s56, 0\n"
    "buffer_load_dwordx4 v236, s[48:51], s65 offen nt lds\n"
    "buffer_load_dwordx4 v237, s[48:51], s65 offen offset:1024 nt lds\n"
    "buffer_load_dwordx4 v238, s[48:51], s65 offen offset:2048 nt lds\n"
    "buffer_load_dwordx4 v239, s[48:51], s65 offen offset:3072 nt lds\n"
    "s_add_u32 m0, s64, 4096\n"
    "s_add_u32 s65, s56, 4096\n"
    "buffer_load_dwordx4 v236, s[48:51], s65 offen nt lds\n"
    "buffer_load_dwordx4 v237, s[48:51], s65 offen offset:1024 nt lds\n"
    "buffer_load_dwordx4 v238, s[48:51], s65 offen offset:2048 nt lds\n"
    "buffer_load_dwordx4 v239, s[48:51], s65 offen offset:3072 nt lds\n"
    "s_add_u32 m0, s64, 8192\n"
    "s_add_u32 s65, s56, 8192\n"
    "buffer_load_dwordx4 v236, s[48:51], s65 offen nt lds\n"
    "buffer_load_dwordx4 v237, s[48:51], s65 offen offset:1024 nt lds\n"
    "buffer_load_dwordx4 v238, s[48:51], s65 offen offset:2048 nt lds\n"
    "buffer_load_dwordx4 v239, s[48:51], s65 offen offset:3072 nt lds\n"
    "s_mov_b64 s[52:53], %[o]\n"
    "s_and_b32 s53, s53, 0xffff\n"
    "s_mov_b32 s54, 65536\n"
    "s_mov_b32 s55, 0x20000\n"
    "v_lshlrev_b32 v247, 4, v242\n"
    "s_and_b32 s93, s93, 0xffff\n"
    "s_mov_b32 s94, Lfin_%=-Lstart_%=\n"
    "s_mov_b32 s95, 0x20000\n"
    "s_lshr_b32 s65, %[bh], 3\n"
    "s_lshl_b32 s65, s65, 2\n"
    "s_add_u32 s65, s65, %[w]\n"
    "s_lshl_b32 s65, s65, 10\n"
    "buffer_load_dwordx4 v[144:147], v247, s[92:95], s65 offen\n"
    "buffer_load_dword v148, v247, s[52:55], s56 offen\n"
    "s_waitcnt lgkmcnt(0)\n"
    "s_mov_b32 s86, s82\n"
    "s_mov_b32 s87, s84\n"
    "s_mov_b64 s[44:45], %[v]\n"
    "s_and_b32 s45, s45, 0xffff\n"
    "s_mov_b32 s46, 524288\n"
    "s_mov_b32 s47, 0x20000\n"
    "v_lshlrev_b32 v247, 1, v244\n"
    "v_xor_b32 v247, v245, v247\n"
    "v_lshlrev_b32 v247, 4, v247\n"
    "v_lshl_add_u32 v240, v244, 8, v247\n"
    "v_xor_b32 v241, 128, v240\n"
    "s_mov_b32 s60, 0x3e0293ee\n"
    "s_mov_b32 s61, 0x3e0293ee\n"
    "s_mov_b32 s62, 0x67800000\n"
    "v_lshl_add_u32 v246, v245, 8, v246\n"
    "v_xor_b32 v224, 0, v246\n"
    "v_add_u32 v224, %[lds], v224\n"
    "v_xor_b32 v225, 64, v246\n"
    "v_add_u32 v225, %[lds], v225\n"
    "v_xor_b32 v226, 128, v246\n"
    "v_add_u32 v226, %[lds], v226\n"
    "v_xor_b32 v227, 192, v246\n"
    "v_add_u32 v227, %[lds], v227\n"
    "v_bfe_u32 v246, v242, 2, 4\n"
    "v_and_b32 v247, 7, v246\n"
    "v_lshlrev_b32 v246, 8, v246\n"
    "v_lshl_add_u32 v246, v247, 5, v246\n"
    "v_and_b32 v247, 3, v242\n"
    "v_lshl_add_u32 v246, v247, 3, v246\n"
    "s_add_u32 s66, %[lds], 65536\n"
    "v_xor_b32 v228, 0, v246\n"
    "v_add_u32 v228, s66, v228\n"
    "v_xor_b32 v229, 32, v246\n"
    "v_add_u32 v229, s66, v229\n"
    "v_xor_b32 v230, 64, v246\n"
    "v_add_u32 v230, s66, v230\n"
    "v_xor_b32 v231, 96, v246\n"
    "v_add_u32 v231, s66, v231\n"
    "v_xor_b32 v232, 128, v246\n"
    "v_add_u32 v232, s66, v232\n"
    "v_xor_b32 v233, 160, v246\n"
    "v_add_u32 v233, s66, v233\n"
    "v_xor_b32 v234, 192, v246\n"
    "v_add_u32 v234, s66, v234\n"
    "v_xor_b32 v235, 224, v246\n"
    "v_add_u32 v235, s66, v235\n"
    "v_and_b32 v246, 3, v242\n"
    "v_cmp_eq_u32 vcc, 0, v246\n"
    "v_mov_b32 v247, 0x3f803f80\n"
    "s_nop 1\n"
    "v_cndmask_b32 v247, 0, v247, vcc\n"
    "v_accvgpr_write_b32 a128, v247\n"
    "v_accvgpr_write_b32 a129, v247\n"
    "v_accvgpr_write_b32 a130, v247\n"
    "v_accvgpr_write_b32 a131, v247\n"
    "v_mov_b32 v0, 0\n"
    "v_mov_b32 v1, 0\n"
    "v_mov_b32 v2, 0\n"
    "v_mov_b32 v3, 0\n"
    "v_mov_b32 v4, 0\n"
    "v_mov_b32 v5, 0\n"
    "v_mov_b32 v6, 0\n"
    "v_mov_b32 v7, 0\n"
    "v_mov_b32 v8, 0\n"
    "v_mov_b32 v9, 0\n"
    "v_mov_b32 v10, 0\n"
    "v_mov_b32 v11, 0\n"
    "v_mov_b32 v12, 0\n"
    "v_mov_b32 v13, 0\n"
    "v_mov_b32 v14, 0\n"
    "v_mov_b32 v15, 0\n"
    "v_mov_b32 v16, 0\n"
    "v_mov_b32 v17, 0\n"
    "v_mov_b32 v18, 0\n"
    "v_mov_b32 v19, 0\n"
    "v_mov_b32 v20, 0\n"
    "v_mov_b32 v21, 0\n"
    "v_mov_b32 v22, 0\n"
    "v_mov_b32 v23, 0\n"
    "v_mov_b32 v24, 0\n"
    "v_mov_b32 v25, 0\n"
    "v_mov_b32 v26, 0\n"
    "v_mov_b32 v27, 0\n"
    "v_mov_b32 v28, 0\n"
    "v_mov_b32 v29, 0\n"
    "v_mov_b32 v30, 0\n"
    "v_mov_b32 v31, 0\n"
    "v_mov_b32 v32, 0\n"
    "v_mov_b32 v33, 0\n"
    "v_mov_b32 v34, 0\n"
    "v_mov_b32 v35, 0\n"
    "v_mov_b32 v36, 0\n"
    "v_mov_b32 v37, 0\n"
    "v_mov_b32 v38, 0\n"
    "v_mov_b32 v39, 0\n"
    "v_mov_b32 v40, 0\n"
    "v_mov_b32 v41, 0\n"
    "v_mov_b32 v42, 0\n"
    "v_mov_b32 v43, 0\n"
    "v_mov_b32 v44, 0\n"
    "v_mov_b32 v45, 0\n"
    "v_mov_b32 v46, 0\n"
    "v_mov_b32 v47, 0\n"
    "v_mov_b32 v48, 0\n"
    "v_mov_b32 v49, 0\n"
    "v_mov_b32 v50, 0\n"
    "v_mov_b32 v51, 0\n"
    "v_mov_b32 v52, 0\n"
    "v_mov_b32 v53, 0\n"
    "v_mov_b32 v54, 0\n"
    "v_mov_b32 v55, 0\n"
    "v_mov_b32 v56, 0\n"
    "v_mov_b32 v57, 0\n"
    "v_mov_b32 v58, 0\n"
    "v_mov_b32 v59, 0\n"
    "v_mov_b32 v60, 0\n"
    "v_mov_b32 v61, 0\n"
    "v_mov_b32 v62, 0\n"
    "v_mov_b32 v63, 0\n"
    "v_mov_b32 v64, 0\n"
    "v_mov_b32 v65, 0\n"
    "v_mov_b32 v66, 0\n"
    "v_mov_b32 v67, 0\n"
    "v_mov_b32 v68, 0\n"
    "v_mov_b32 v69, 0\n"
    "v_mov_b32 v70, 0\n"
    "v_mov_b32 v71, 0\n"
    "v_mov_b32 v72, 0\n"
    "v_mov_b32 v73, 0\n"
    "v_mov_b32 v74, 0\n"
    "v_mov_b32 v75, 0\n"
    "v_mov_b32 v76, 0\n"
    "v_mov_b32 v77, 0\n"
    "v_mov_b32 v78, 0\n"
    "v_mov_b32 v79, 0\n"
    "v_mov_b32 v80, 0\n"
    "v_mov_b32 v81, 0\n"
    "v_mov_b32 v82, 0\n"
    "v_mov_b32 v83, 0\n"
    "v_mov_b32 v84, 0\n"
    "v_mov_b32 v85, 0\n"
    "v_mov_b32 v86, 0\n"
    "v_mov_b32 v87, 0\n"
    "v_mov_b32 v88, 0\n"
    "v_mov_b32 v89, 0\n"
    "v_mov_b32 v90, 0\n"
    "v_mov_b32 v91, 0\n"
    "v_mov_b32 v92, 0\n"
    "v_mov_b32 v93, 0\n"
    "v_mov_b32 v94, 0\n"
    "v_mov_b32 v95, 0\n"
    "v_mov_b32 v96, 0\n"
    "v_mov_b32 v97, 0\n"
    "v_mov_b32 v98, 0\n"
    "v_mov_b32 v99, 0\n"
    "v_mov_b32 v100, 0\n"
    "v_mov_b32 v101, 0\n"
    "v_mov_b32 v102, 0\n"
    "v_mov_b32 v103, 0\n"
    "v_mov_b32 v104, 0\n"
    "v_mov_b32 v105, 0\n"
    "v_mov_b32 v106, 0\n"
    "v_mov_b32 v107, 0\n"
    "v_mov_b32 v108, 0\n"
    "v_mov_b32 v109, 0\n"
    "v_mov_b32 v110, 0\n"
    "v_mov_b32 v111, 0\n"
    "v_mov_b32 v112, 0\n"
    "v_mov_b32 v113, 0\n"
    "v_mov_b32 v114, 0\n"
    "v_mov_b32 v115, 0\n"
    "v_mov_b32 v116, 0\n"
    "v_mov_b32 v117, 0\n"
    "v_mov_b32 v118, 0\n"
    "v_mov_b32 v119, 0\n"
    "v_mov_b32 v120, 0\n"
    "v_mov_b32 v121, 0\n"
    "v_mov_b32 v122, 0\n"
    "v_mov_b32 v123, 0\n"
    "v_mov_b32 v124, 0\n"
    "v_mov_b32 v125, 0\n"
    "v_mov_b32 v126, 0\n"
    "v_mov_b32 v127, 0\n"
    "v_mov_b32 v128, 0\n"
    "v_mov_b32 v129, 0\n"
    "v_mov_b32 v130, 0\n"
    "v_mov_b32 v131, 0\n"
    "v_mov_b32 v132, 0\n"
    "v_mov_b32 v133, 0\n"
    "v_mov_b32 v134, 0\n"
    "v_mov_b32 v135, 0\n"
    "v_mov_b32 v136, 0\n"
    "v_mov_b32 v137, 0\n"
    "v_mov_b32 v138, 0\n"
    "v_mov_b32 v139, 0\n"
    "v_mov_b32 v140, 0\n"
    "v_mov_b32 v141, 0\n"
    "v_mov_b32 v142, 0\n"
    "v_mov_b32 v143, 0\n"
    "v_add_u32 v244, s63, v224\n"
    "v_add_u32 v245, s63, v225\n"
    "v_add_u32 v246, s63, v226\n"
    "v_add_u32 v247, s63, v227\n"
    "s_add_u32 m0, s64, 12288\n"
    "s_add_u32 s65, s56, 12288\n"
    "buffer_load_dwordx4 v236, s[48:51], s65 offen nt lds\n"
    "buffer_load_dwordx4 v237, s[48:51], s65 offen offset:1024 nt lds\n"
    "buffer_load_dwordx4 v238, s[48:51], s65 offen offset:2048 nt lds\n"
    "buffer_load_dwordx4 v239, s[48:51], s65 offen offset:3072 nt lds\n"
    "s_waitcnt vmcnt(14)\n"
    "s_barrier\n"
    "ds_read_b128 v[176:179], v244\n"
    "ds_read_b128 v[180:183], v245\n"
    "ds_read_b128 v[184:187], v246\n"
    "ds_read_b128 v[188:191], v247\n"
    "ds_read_b128 a[64:67], v224\n"
    "ds_read_b128 a[68:71], v225\n"
    "ds_read_b128 a[72:75], v226\n"
    "ds_read_b128 a[76:79], v227\n"
    "ds_read_b128 a[80:83], v224 offset:4096\n"
    "ds_read_b128 a[84:87], v225 offset:4096\n"
    "ds_read_b128 a[88:91], v226 offset:4096\n"
    "ds_read_b128 a[92:95], v227 offset:4096\n"
    "s_waitcnt lgkmcnt(8)\n"
    "v_lshlrev_b32 v248, 16, v176\n"
    "v_and_b32 v249, 0xffff0000, v176\n"
    "v_pk_mul_f32 v[248:249], v[248:249], s[60:61]\n"
    "v_cvt_pk_bf16_f32 v176, v248, v249\n"
    "v_accvgpr_write_b32 a0, v176\n"
    "v_lshlrev_b32 v250, 16, v177\n"
    "v_and_b32 v251, 0xffff0000, v177\n"
    "v_pk_mul_f32 v[250:251], v[250:251], s[60:61]\n"
    "v_cvt_pk_bf16_f32 v177, v250, v251\n"
    "v_accvgpr_write_b32 a1, v177\n"
    "v_lshlrev_b32 v248, 16, v178\n"
    "v_and_b32 v249, 0xffff0000, v178\n"
    "v_pk_mul_f32 v[248:249], v[248:249], s[60:61]\n"
    "v_cvt_pk_bf16_f32 v178, v248, v249\n"
    "v_accvgpr_write_b32 a2, v178\n"
    "v_lshlrev_b32 v250, 16, v179\n"
    "v_and_b32 v251, 0xffff0000, v179\n"
    "v_pk_mul_f32 v[250:251], v[250:251], s[60:61]\n"
    "v_cvt_pk_bf16_f32 v179, v250, v251\n"
    "v_accvgpr_write_b32 a3, v179\n"
    "v_lshlrev_b32 v248, 16, v180\n"
    "v_and_b32 v249, 0xffff0000, v180\n"
    "v_pk_mul_f32 v[248:249], v[248:249], s[60:61]\n"
    "v_cvt_pk_bf16_f32 v180, v248, v249\n"
    "v_accvgpr_write_b32 a4, v180\n"
    "v_lshlrev_b32 v250, 16, v181\n"
    "v_and_b32 v251, 0xffff0000, v181\n"
    "v_pk_mul_f32 v[250:251], v[250:251], s[60:61]\n"
    "v_cvt_pk_bf16_f32 v181, v250, v251\n"
    "v_accvgpr_write_b32 a5, v181\n"
    "v_lshlrev_b32 v248, 16, v182\n"
    "v_and_b32 v249, 0xffff0000, v182\n"
    "v_pk_mul_f32 v[248:249], v[248:249], s[60:61]\n"
    "v_cvt_pk_bf16_f32 v182, v248, v249\n"
    "v_accvgpr_write_b32 a6, v182\n"
    "v_lshlrev_b32 v250, 16, v183\n"
    "v_and_b32 v251, 0xffff0000, v183\n"
    "v_pk_mul_f32 v[250:251], v[250:251], s[60:61]\n"
    "v_cvt_pk_bf16_f32 v183, v250, v251\n"
    "v_accvgpr_write_b32 a7, v183\n"
    "v_lshlrev_b32 v248, 16, v184\n"
    "v_and_b32 v249, 0xffff0000, v184\n"
    "v_pk_mul_f32 v[248:249], v[248:249], s[60:61]\n"
    "v_cvt_pk_bf16_f32 v184, v248, v249\n"
    "v_accvgpr_write_b32 a8, v184\n"
    "v_lshlrev_b32 v250, 16, v185\n"
    "v_and_b32 v251, 0xffff0000, v185\n"
    "v_pk_mul_f32 v[250:251], v[250:251], s[60:61]\n"
    "v_cvt_pk_bf16_f32 v185, v250, v251\n"
    "v_accvgpr_write_b32 a9, v185\n"
    "v_lshlrev_b32 v248, 16, v186\n"
    "v_and_b32 v249, 0xffff0000, v186\n"
    "v_pk_mul_f32 v[248:249], v[248:249], s[60:61]\n"
    "v_cvt_pk_bf16_f32 v186, v248, v249\n"
    "v_accvgpr_write_b32 a10, v186\n"
    "v_lshlrev_b32 v250, 16, v187\n"
    "v_and_b32 v251, 0xffff0000, v187\n"
    "v_pk_mul_f32 v[250:251], v[250:251], s[60:61]\n"
    "v_cvt_pk_bf16_f32 v187, v250, v251\n"
    "v_accvgpr_write_b32 a11, v187\n"
    "v_lshlrev_b32 v248, 16, v188\n"
    "v_and_b32 v249, 0xffff0000, v188\n"
    "v_pk_mul_f32 v[248:249], v[248:249], s[60:61]\n"
    "v_cvt_pk_bf16_f32 v188, v248, v249\n"
    "v_accvgpr_write_b32 a12, v188\n"
    "v_lshlrev_b32 v250, 16, v189\n"
    "v_and_b32 v251, 0xffff0000, v189\n"
    "v_pk_mul_f32 v[250:251], v[250:251], s[60:61]\n"
    "v_cvt_pk_bf16_f32 v189, v250, v251\n"
    "v_accvgpr_write_b32 a13, v189\n"
    "v_lshlrev_b32 v248, 16, v190\n"
    "v_and_b32 v249, 0xffff0000, v190\n"
    "v_pk_mul_f32 v[248:249], v[248:249], s[60:61]\n"
    "v_cvt_pk_bf16_f32 v190, v248, v249\n"
    "v_accvgpr_write_b32 a14, v190\n"
    "v_lshlrev_b32 v250, 16, v191\n"
    "v_and_b32 v251, 0xffff0000, v191\n"
    "v_pk_mul_f32 v[250:251], v[250:251], s[60:61]\n"
    "v_cvt_pk_bf16_f32 v191, v250, v251\n"
    "v_accvgpr_write_b32 a15, v191\n"
    "s_waitcnt lgkmcnt(0)\n"
    "s_nop 1\n"
    "v_mfma_f32_16x16x32_bf16 v[160:163], a[64:67], a[0:3], 0\n"
    "v_mfma_f32_16x16x32_bf16 v[160:163], a[68:71], a[4:7], v[160:163]\n"
    "v_mfma_f32_16x16x32_bf16 v[160:163], a[72:75], a[8:11], v[160:163]\n"
    "v_mfma_f32_16x16x32_bf16 v[160:163], a[76:79], a[12:15], v[160:163]\n"
    "s_waitcnt vmcnt(10)\n"
    "ds_read_b128 v[176:179], v244 offset:4096\n"
    "ds_read_b128 v[180:183], v245 offset:4096\n"
    "ds_read_b128 v[184:187], v246 offset:4096\n"
    "ds_read_b128 v[188:191], v247 offset:4096\n"
    "s_waitcnt lgkmcnt(0)\n"
    "v_lshlrev_b32 v248, 16, v176\n"
    "v_and_b32 v249, 0xffff0000, v176\n"
    "v_pk_mul_f32 v[248:249], v[248:249], s[60:61]\n"
    "v_cvt_pk_bf16_f32 v176, v248, v249\n"
    "v_accvgpr_write_b32 a16, v176\n"
    "v_lshlrev_b32 v250, 16, v177\n"
    "v_and_b32 v251, 0xffff0000, v177\n"
    "v_pk_mul_f32 v[250:251], v[250:251], s[60:61]\n"
    "v_cvt_pk_bf16_f32 v177, v250, v251\n"
    "v_accvgpr_write_b32 a17, v177\n"
    "v_lshlrev_b32 v248, 16, v178\n"
    "v_and_b32 v249, 0xffff0000, v178\n"
    "v_pk_mul_f32 v[248:249], v[248:249], s[60:61]\n"
    "v_cvt_pk_bf16_f32 v178, v248, v249\n"
    "v_accvgpr_write_b32 a18, v178\n"
    "v_lshlrev_b32 v250, 16, v179\n"
    "v_and_b32 v251, 0xffff0000, v179\n"
    "v_pk_mul_f32 v[250:251], v[250:251], s[60:61]\n"
    "v_cvt_pk_bf16_f32 v179, v250, v251\n"
    "v_accvgpr_write_b32 a19, v179\n"
    "v_lshlrev_b32 v248, 16, v180\n"
    "v_and_b32 v249, 0xffff0000, v180\n"
    "v_pk_mul_f32 v[248:249], v[248:249], s[60:61]\n"
    "v_cvt_pk_bf16_f32 v180, v248, v249\n"
    "v_accvgpr_write_b32 a20, v180\n"
    "v_lshlrev_b32 v250, 16, v181\n"
    "v_and_b32 v251, 0xffff0000, v181\n"
    "v_pk_mul_f32 v[250:251], v[250:251], s[60:61]\n"
    "v_cvt_pk_bf16_f32 v181, v250, v251\n"
    "v_accvgpr_write_b32 a21, v181\n"
    "v_lshlrev_b32 v248, 16, v182\n"
    "v_and_b32 v249, 0xffff0000, v182\n"
    "v_pk_mul_f32 v[248:249], v[248:249], s[60:61]\n"
    "v_cvt_pk_bf16_f32 v182, v248, v249\n"
    "v_accvgpr_write_b32 a22, v182\n"
    "v_lshlrev_b32 v250, 16, v183\n"
    "v_and_b32 v251, 0xffff0000, v183\n"
    "v_pk_mul_f32 v[250:251], v[250:251], s[60:61]\n"
    "v_cvt_pk_bf16_f32 v183, v250, v251\n"
    "v_accvgpr_write_b32 a23, v183\n"
    "v_lshlrev_b32 v248, 16, v184\n"
    "v_and_b32 v249, 0xffff0000, v184\n"
    "v_pk_mul_f32 v[248:249], v[248:249], s[60:61]\n"
    "v_cvt_pk_bf16_f32 v184, v248, v249\n"
    "v_accvgpr_write_b32 a24, v184\n"
    "v_lshlrev_b32 v250, 16, v185\n"
    "v_and_b32 v251, 0xffff0000, v185\n"
    "v_pk_mul_f32 v[250:251], v[250:251], s[60:61]\n"
    "v_cvt_pk_bf16_f32 v185, v250, v251\n"
    "v_accvgpr_write_b32 a25, v185\n"
    "v_lshlrev_b32 v248, 16, v186\n"
    "v_and_b32 v249, 0xffff0000, v186\n"
    "v_pk_mul_f32 v[248:249], v[248:249], s[60:61]\n"
    "v_cvt_pk_bf16_f32 v186, v248, v249\n"
    "v_accvgpr_write_b32 a26, v186\n"
    "v_lshlrev_b32 v250, 16, v187\n"
    "v_and_b32 v251, 0xffff0000, v187\n"
    "v_pk_mul_f32 v[250:251], v[250:251], s[60:61]\n"
    "v_cvt_pk_bf16_f32 v187, v250, v251\n"
    "v_accvgpr_write_b32 a27, v187\n"
    "v_lshlrev_b32 v248, 16, v188\n"
    "v_and_b32 v249, 0xffff0000, v188\n"
    "v_pk_mul_f32 v[248:249], v[248:249], s[60:61]\n"
    "v_cvt_pk_bf16_f32 v188, v248, v249\n"
    "v_accvgpr_write_b32 a28, v188\n"
    "v_lshlrev_b32 v250, 16, v189\n"
    "v_and_b32 v251, 0xffff0000, v189\n"
    "v_pk_mul_f32 v[250:251], v[250:251], s[60:61]\n"
    "v_cvt_pk_bf16_f32 v189, v250, v251\n"
    "v_accvgpr_write_b32 a29, v189\n"
    "v_lshlrev_b32 v248, 16, v190\n"
    "v_and_b32 v249, 0xffff0000, v190\n"
    "v_pk_mul_f32 v[248:249], v[248:249], s[60:61]\n"
    "v_cvt_pk_bf16_f32 v190, v248, v249\n"
    "v_accvgpr_write_b32 a30, v190\n"
    "v_lshlrev_b32 v250, 16, v191\n"
    "v_and_b32 v251, 0xffff0000, v191\n"
    "v_pk_mul_f32 v[250:251], v[250:251], s[60:61]\n"
    "v_cvt_pk_bf16_f32 v191, v250, v251\n"
    "v_accvgpr_write_b32 a31, v191\n"
    "s_add_u32 m0, s57, 65536\n"
    "s_add_u32 s65, s59, 0\n"
    "buffer_load_dwordx4 v240, s[44:47], s65 offen nt lds\n"
    "buffer_load_dwordx4 v241, s[44:47], s65 offen offset:1024 nt lds\n"
    "buffer_load_dwordx4 v240, s[44:47], s65 offen offset:2048 nt lds\n"
    "buffer_load_dwordx4 v241, s[44:47], s65 offen offset:3072 nt lds\n"
    "v_mfma_f32_16x16x32_bf16 v[164:167], a[64:67], a[16:19], 0\n"
    "v_mfma_f32_16x16x32_bf16 v[164:167], a[68:71], a[20:23], v[164:167]\n"
    "v_mfma_f32_16x16x32_bf16 v[164:167], a[72:75], a[24:27], v[164:167]\n"
    "v_mfma_f32_16x16x32_bf16 v[164:167], a[76:79], a[28:31], v[164:167]\n"
    "s_waitcnt vmcnt(10)\n"
    "ds_read_b128 v[176:179], v244 offset:8192\n"
    "ds_read_b128 v[180:183], v245 offset:8192\n"
    "ds_read_b128 v[184:187], v246 offset:8192\n"
    "ds_read_b128 v[188:191], v247 offset:8192\n"
    "s_waitcnt lgkmcnt(0)\n"
    "v_lshlrev_b32 v248, 16, v176\n"
    "v_and_b32 v249, 0xffff0000, v176\n"
    "v_pk_mul_f32 v[248:249], v[248:249], s[60:61]\n"
    "v_cvt_pk_bf16_f32 v176, v248, v249\n"
    "v_accvgpr_write_b32 a32, v176\n"
    "v_lshlrev_b32 v250, 16, v177\n"
    "v_and_b32 v251, 0xffff0000, v177\n"
    "v_pk_mul_f32 v[250:251], v[250:251], s[60:61]\n"
    "v_cvt_pk_bf16_f32 v177, v250, v251\n"
    "v_accvgpr_write_b32 a33, v177\n"
    "v_lshlrev_b32 v248, 16, v178\n"
    "v_and_b32 v249, 0xffff0000, v178\n"
    "v_pk_mul_f32 v[248:249], v[248:249], s[60:61]\n"
    "v_cvt_pk_bf16_f32 v178, v248, v249\n"
    "v_accvgpr_write_b32 a34, v178\n"
    "v_lshlrev_b32 v250, 16, v179\n"
    "v_and_b32 v251, 0xffff0000, v179\n"
    "v_pk_mul_f32 v[250:251], v[250:251], s[60:61]\n"
    "v_cvt_pk_bf16_f32 v179, v250, v251\n"
    "v_accvgpr_write_b32 a35, v179\n"
    "v_lshlrev_b32 v248, 16, v180\n"
    "v_and_b32 v249, 0xffff0000, v180\n"
    "v_pk_mul_f32 v[248:249], v[248:249], s[60:61]\n"
    "v_cvt_pk_bf16_f32 v180, v248, v249\n"
    "v_accvgpr_write_b32 a36, v180\n"
    "v_lshlrev_b32 v250, 16, v181\n"
    "v_and_b32 v251, 0xffff0000, v181\n"
    "v_pk_mul_f32 v[250:251], v[250:251], s[60:61]\n"
    "v_cvt_pk_bf16_f32 v181, v250, v251\n"
    "v_accvgpr_write_b32 a37, v181\n"
    "v_lshlrev_b32 v248, 16, v182\n"
    "v_and_b32 v249, 0xffff0000, v182\n"
    "v_pk_mul_f32 v[248:249], v[248:249], s[60:61]\n"
    "v_cvt_pk_bf16_f32 v182, v248, v249\n"
    "v_accvgpr_write_b32 a38, v182\n"
    "v_lshlrev_b32 v250, 16, v183\n"
    "v_and_b32 v251, 0xffff0000, v183\n"
    "v_pk_mul_f32 v[250:251], v[250:251], s[60:61]\n"
    "v_cvt_pk_bf16_f32 v183, v250, v251\n"
    "v_accvgpr_write_b32 a39, v183\n"
    "v_lshlrev_b32 v248, 16, v184\n"
    "v_and_b32 v249, 0xffff0000, v184\n"
    "v_pk_mul_f32 v[248:249], v[248:249], s[60:61]\n"
    "v_cvt_pk_bf16_f32 v184, v248, v249\n"
    "v_accvgpr_write_b32 a40, v184\n"
    "v_lshlrev_b32 v250, 16, v185\n"
    "v_and_b32 v251, 0xffff0000, v185\n"
    "v_pk_mul_f32 v[250:251], v[250:251], s[60:61]\n"
    "v_cvt_pk_bf16_f32 v185, v250, v251\n"
    "v_accvgpr_write_b32 a41, v185\n"
    "v_lshlrev_b32 v248, 16, v186\n"
    "v_and_b32 v249, 0xffff0000, v186\n"
    "v_pk_mul_f32 v[248:249], v[248:249], s[60:61]\n"
    "v_cvt_pk_bf16_f32 v186, v248, v249\n"
    "v_accvgpr_write_b32 a42, v186\n"
    "v_lshlrev_b32 v250, 16, v187\n"
    "v_and_b32 v251, 0xffff0000, v187\n"
    "v_pk_mul_f32 v[250:251], v[250:251], s[60:61]\n"
    "v_cvt_pk_bf16_f32 v187, v250, v251\n"
    "v_accvgpr_write_b32 a43, v187\n"
    "v_lshlrev_b32 v248, 16, v188\n"
    "v_and_b32 v249, 0xffff0000, v188\n"
    "v_pk_mul_f32 v[248:249], v[248:249], s[60:61]\n"
    "v_cvt_pk_bf16_f32 v188, v248, v249\n"
    "v_accvgpr_write_b32 a44, v188\n"
    "v_lshlrev_b32 v250, 16, v189\n"
    "v_and_b32 v251, 0xffff0000, v189\n"
    "v_pk_mul_f32 v[250:251], v[250:251], s[60:61]\n"
    "v_cvt_pk_bf16_f32 v189, v250, v251\n"
    "v_accvgpr_write_b32 a45, v189\n"
    "v_lshlrev_b32 v248, 16, v190\n"
    "v_and_b32 v249, 0xffff0000, v190\n"
    "v_pk_mul_f32 v[248:249], v[248:249], s[60:61]\n"
    "v_cvt_pk_bf16_f32 v190, v248, v249\n"
    "v_accvgpr_write_b32 a46, v190\n"
    "v_lshlrev_b32 v250, 16, v191\n"
    "v_and_b32 v251, 0xffff0000, v191\n"
    "v_pk_mul_f32 v[250:251], v[250:251], s[60:61]\n"
    "v_cvt_pk_bf16_f32 v191, v250, v251\n"
    "v_accvgpr_write_b32 a47, v191\n"
    "s_add_u32 m0, s57, 16384\n"
    "s_add_u32 s65, s59, 16384\n"
    "buffer_load_dwordx4 v236, s[40:43], s65 offen nt lds\n"
    "buffer_load_dwordx4 v237, s[40:43], s65 offen offset:1024 nt lds\n"
    "buffer_load_dwordx4 v238, s[40:43], s65 offen offset:2048 nt lds\n"
    "buffer_load_dwordx4 v239, s[40:43], s65 offen offset:3072 nt lds\n"
    "v_mfma_f32_16x16x32_bf16 v[168:171], a[64:67], a[32:35], 0\n"
    "v_mfma_f32_16x16x32_bf16 v[168:171], a[68:71], a[36:39], v[168:171]\n"
    "v_mfma_f32_16x16x32_bf16 v[168:171], a[72:75], a[40:43], v[168:171]\n"
    "v_mfma_f32_16x16x32_bf16 v[168:171], a[76:79], a[44:47], v[168:171]\n"
    "s_waitcnt vmcnt(8)\n"
    "ds_read_b128 v[176:179], v244 offset:12288\n"
    "ds_read_b128 v[180:183], v245 offset:12288\n"
    "ds_read_b128 v[184:187], v246 offset:12288\n"
    "ds_read_b128 v[188:191], v247 offset:12288\n"
    "s_waitcnt lgkmcnt(0)\n"
    "v_lshlrev_b32 v248, 16, v176\n"
    "v_and_b32 v249, 0xffff0000, v176\n"
    "v_pk_mul_f32 v[248:249], v[248:249], s[60:61]\n"
    "v_cvt_pk_bf16_f32 v176, v248, v249\n"
    "v_accvgpr_write_b32 a48, v176\n"
    "v_lshlrev_b32 v250, 16, v177\n"
    "v_and_b32 v251, 0xffff0000, v177\n"
    "v_pk_mul_f32 v[250:251], v[250:251], s[60:61]\n"
    "v_cvt_pk_bf16_f32 v177, v250, v251\n"
    "v_accvgpr_write_b32 a49, v177\n"
    "v_lshlrev_b32 v248, 16, v178\n"
    "v_and_b32 v249, 0xffff0000, v178\n"
    "v_pk_mul_f32 v[248:249], v[248:249], s[60:61]\n"
    "v_cvt_pk_bf16_f32 v178, v248, v249\n"
    "v_accvgpr_write_b32 a50, v178\n"
    "v_lshlrev_b32 v250, 16, v179\n"
    "v_and_b32 v251, 0xffff0000, v179\n"
    "v_pk_mul_f32 v[250:251], v[250:251], s[60:61]\n"
    "v_cvt_pk_bf16_f32 v179, v250, v251\n"
    "v_accvgpr_write_b32 a51, v179\n"
    "v_lshlrev_b32 v248, 16, v180\n"
    "v_and_b32 v249, 0xffff0000, v180\n"
    "v_pk_mul_f32 v[248:249], v[248:249], s[60:61]\n"
    "v_cvt_pk_bf16_f32 v180, v248, v249\n"
    "v_accvgpr_write_b32 a52, v180\n"
    "v_lshlrev_b32 v250, 16, v181\n"
    "v_and_b32 v251, 0xffff0000, v181\n"
    "v_pk_mul_f32 v[250:251], v[250:251], s[60:61]\n"
    "v_cvt_pk_bf16_f32 v181, v250, v251\n"
    "v_accvgpr_write_b32 a53, v181\n"
    "v_lshlrev_b32 v248, 16, v182\n"
    "v_and_b32 v249, 0xffff0000, v182\n"
    "v_pk_mul_f32 v[248:249], v[248:249], s[60:61]\n"
    "v_cvt_pk_bf16_f32 v182, v248, v249\n"
    "v_accvgpr_write_b32 a54, v182\n"
    "v_lshlrev_b32 v250, 16, v183\n"
    "v_and_b32 v251, 0xffff0000, v183\n"
    "v_pk_mul_f32 v[250:251], v[250:251], s[60:61]\n"
    "v_cvt_pk_bf16_f32 v183, v250, v251\n"
    "v_accvgpr_write_b32 a55, v183\n"
    "v_lshlrev_b32 v248, 16, v184\n"
    "v_and_b32 v249, 0xffff0000, v184\n"
    "v_pk_mul_f32 v[248:249], v[248:249], s[60:61]\n"
    "v_cvt_pk_bf16_f32 v184, v248, v249\n"
    "v_accvgpr_write_b32 a56, v184\n"
    "v_lshlrev_b32 v250, 16, v185\n"
    "v_and_b32 v251, 0xffff0000, v185\n"
    "v_pk_mul_f32 v[250:251], v[250:251], s[60:61]\n"
    "v_cvt_pk_bf16_f32 v185, v250, v251\n"
    "v_accvgpr_write_b32 a57, v185\n"
    "v_lshlrev_b32 v248, 16, v186\n"
    "v_and_b32 v249, 0xffff0000, v186\n"
    "v_pk_mul_f32 v[248:249], v[248:249], s[60:61]\n"
    "v_cvt_pk_bf16_f32 v186, v248, v249\n"
    "v_accvgpr_write_b32 a58, v186\n"
    "v_lshlrev_b32 v250, 16, v187\n"
    "v_and_b32 v251, 0xffff0000, v187\n"
    "v_pk_mul_f32 v[250:251], v[250:251], s[60:61]\n"
    "v_cvt_pk_bf16_f32 v187, v250, v251\n"
    "v_accvgpr_write_b32 a59, v187\n"
    "v_lshlrev_b32 v248, 16, v188\n"
    "v_and_b32 v249, 0xffff0000, v188\n"
    "v_pk_mul_f32 v[248:249], v[248:249], s[60:61]\n"
    "v_cvt_pk_bf16_f32 v188, v248, v249\n"
    "v_accvgpr_write_b32 a60, v188\n"
    "v_lshlrev_b32 v250, 16, v189\n"
    "v_and_b32 v251, 0xffff0000, v189\n"
    "v_pk_mul_f32 v[250:251], v[250:251], s[60:61]\n"
    "v_cvt_pk_bf16_f32 v189, v250, v251\n"
    "v_accvgpr_write_b32 a61, v189\n"
    "v_lshlrev_b32 v248, 16, v190\n"
    "v_and_b32 v249, 0xffff0000, v190\n"
    "v_pk_mul_f32 v[248:249], v[248:249], s[60:61]\n"
    "v_cvt_pk_bf16_f32 v190, v248, v249\n"
    "v_accvgpr_write_b32 a62, v190\n"
    "v_lshlrev_b32 v250, 16, v191\n"
    "v_and_b32 v251, 0xffff0000, v191\n"
    "v_pk_mul_f32 v[250:251], v[250:251], s[60:61]\n"
    "v_cvt_pk_bf16_f32 v191, v250, v251\n"
    "v_accvgpr_write_b32 a63, v191\n"
    "s_add_u32 m0, s57, 81920\n"
    "s_add_u32 s65, s59, 16384\n"
    "buffer_load_dwordx4 v240, s[44:47], s65 offen nt lds\n"
    "buffer_load_dwordx4 v241, s[44:47], s65 offen offset:1024 nt lds\n"
    "buffer_load_dwordx4 v240, s[44:47], s65 offen offset:2048 nt lds\n"
    "buffer_load_dwordx4 v241, s[44:47], s65 offen offset:3072 nt lds\n"
    "v_mfma_f32_16x16x32_bf16 v[172:175], a[64:67], a[48:51], 0\n"
    "v_mfma_f32_16x16x32_bf16 v[172:175], a[68:71], a[52:55], v[172:175]\n"
    "v_mfma_f32_16x16x32_bf16 v[172:175], a[72:75], a[56:59], v[172:175]\n"
    "v_mfma_f32_16x16x32_bf16 v[172:175], a[76:79], a[60:63], v[172:175]\n"
    "s_add_u32 s56, s59, 49152\n"
    "ds_read_b128 a[64:67], v224 offset:8192\n"
    "ds_read_b128 a[68:71], v225 offset:8192\n"
    "ds_read_b128 a[72:75], v226 offset:8192\n"
    "ds_read_b128 a[76:79], v227 offset:8192\n"
    "s_nop 7\n"
    "s_nop 3\n"
    "v_max3_f32 v244, v160, v161, v162\n"
    "v_max_f32 v244, v244, v163\n"
    "v_mov_b32 v248, v244\n"
    "v_max3_f32 v245, v164, v165, v166\n"
    "v_max_f32 v245, v245, v167\n"
    "v_mov_b32 v249, v245\n"
    "v_max3_f32 v246, v168, v169, v170\n"
    "v_max_f32 v246, v246, v171\n"
    "v_mov_b32 v250, v246\n"
    "v_max3_f32 v247, v172, v173, v174\n"
    "v_max_f32 v247, v247, v175\n"
    "v_mov_b32 v251, v247\n"
    "s_nop 1\n"
    "v_permlane16_swap_b32 v244, v248\n"
    "v_permlane16_swap_b32 v245, v249\n"
    "v_permlane16_swap_b32 v246, v250\n"
    "v_permlane16_swap_b32 v247, v251\n"
    "s_nop 1\n"
    "v_max_f32 v244, v244, v248\n"
    "v_max_f32 v245, v245, v249\n"
    "v_max_f32 v246, v246, v250\n"
    "v_max_f32 v247, v247, v251\n"
    "v_mov_b32 v248, v244\n"
    "v_mov_b32 v249, v245\n"
    "v_mov_b32 v250, v246\n"
    "v_mov_b32 v251, v247\n"
    "s_nop 1\n"
    "v_permlane32_swap_b32 v244, v248\n"
    "v_permlane32_swap_b32 v245, v249\n"
    "v_permlane32_swap_b32 v246, v250\n"
    "v_permlane32_swap_b32 v247, v251\n"
    "s_nop 1\n"
    "v_max_f32 v244, v244, v248\n"
    "v_max_f32 v245, v245, v249\n"
    "v_max_f32 v246, v246, v250\n"
    "v_max_f32 v247, v247, v251\n"
    "v_sub_f32 v144, 0, v244\n"
    "v_mov_b32 v145, v144\n"
    "v_mov_b32 v146, v144\n"
    "v_mov_b32 v147, v144\n"
    "v_add_f32 v160, v160, v144\n"
    "v_add_f32 v161, v161, v144\n"
    "v_add_f32 v162, v162, v144\n"
    "v_add_f32 v163, v163, v144\n"
    "v_sub_f32 v148, 0, v245\n"
    "v_mov_b32 v149, v148\n"
    "v_mov_b32 v150, v148\n"
    "v_mov_b32 v151, v148\n"
    "v_add_f32 v164, v164, v148\n"
    "v_add_f32 v165, v165, v148\n"
    "v_add_f32 v166, v166, v148\n"
    "v_add_f32 v167, v167, v148\n"
    "v_sub_f32 v152, 0, v246\n"
    "v_mov_b32 v153, v152\n"
    "v_mov_b32 v154, v152\n"
    "v_mov_b32 v155, v152\n"
    "v_add_f32 v168, v168, v152\n"
    "v_add_f32 v169, v169, v152\n"
    "v_add_f32 v170, v170, v152\n"
    "v_add_f32 v171, v171, v152\n"
    "v_sub_f32 v156, 0, v247\n"
    "v_mov_b32 v157, v156\n"
    "v_mov_b32 v158, v156\n"
    "v_mov_b32 v159, v156\n"
    "v_add_f32 v172, v172, v156\n"
    "v_add_f32 v173, v173, v156\n"
    "v_add_f32 v174, v174, v156\n"
    "v_add_f32 v175, v175, v156\n"
    "v_mfma_f32_16x16x32_bf16 v[176:179], a[80:83], a[0:3], v[144:147]\n"
    "v_exp_f32 v160, v160\n"
    "v_mfma_f32_16x16x32_bf16 v[176:179], a[84:87], a[4:7], v[176:179]\n"
    "v_exp_f32 v161, v161\n"
    "v_mfma_f32_16x16x32_bf16 v[176:179], a[88:91], a[8:11], v[176:179]\n"
    "v_exp_f32 v162, v162\n"
    "v_cvt_pk_bf16_f32 v192, v160, v161\n"
    "v_mfma_f32_16x16x32_bf16 v[176:179], a[92:95], a[12:15], v[176:179]\n"
    "v_exp_f32 v163, v163\n"
    "v_mfma_f32_16x16x32_bf16 v[180:183], a[80:83], a[16:19], v[148:151]\n"
    "v_exp_f32 v164, v164\n"
    "v_cvt_pk_bf16_f32 v193, v162, v163\n"
    "v_mfma_f32_16x16x32_bf16 v[180:183], a[84:87], a[20:23], v[180:183]\n"
    "v_exp_f32 v165, v165\n"
    "v_mfma_f32_16x16x32_bf16 v[180:183], a[88:91], a[24:27], v[180:183]\n"
    "v_exp_f32 v166, v166\n"
    "v_cvt_pk_bf16_f32 v196, v164, v165\n"
    "v_mfma_f32_16x16x32_bf16 v[180:183], a[92:95], a[28:31], v[180:183]\n"
    "v_exp_f32 v167, v167\n"
    "v_mfma_f32_16x16x32_bf16 v[184:187], a[80:83], a[32:35], v[152:155]\n"
    "v_exp_f32 v168, v168\n"
    "v_cvt_pk_bf16_f32 v197, v166, v167\n"
    "v_mfma_f32_16x16x32_bf16 v[184:187], a[84:87], a[36:39], v[184:187]\n"
    "v_exp_f32 v169, v169\n"
    "v_mfma_f32_16x16x32_bf16 v[184:187], a[88:91], a[40:43], v[184:187]\n"
    "v_exp_f32 v170, v170\n"
    "v_cvt_pk_bf16_f32 v200, v168, v169\n"
    "v_mfma_f32_16x16x32_bf16 v[184:187], a[92:95], a[44:47], v[184:187]\n"
    "v_exp_f32 v171, v171\n"
    "v_mfma_f32_16x16x32_bf16 v[188:191], a[80:83], a[48:51], v[156:159]\n"
    "v_exp_f32 v172, v172\n"
    "v_cvt_pk_bf16_f32 v201, v170, v171\n"
    "v_mfma_f32_16x16x32_bf16 v[188:191], a[84:87], a[52:55], v[188:191]\n"
    "v_exp_f32 v173, v173\n"
    "v_mfma_f32_16x16x32_bf16 v[188:191], a[88:91], a[56:59], v[188:191]\n"
    "v_exp_f32 v174, v174\n"
    "v_cvt_pk_bf16_f32 v204, v172, v173\n"
    "v_mfma_f32_16x16x32_bf16 v[188:191], a[92:95], a[60:63], v[188:191]\n"
    "v_exp_f32 v175, v175\n"
    "s_nop 0\n"
    "v_cvt_pk_bf16_f32 v205, v174, v175\n"
    "s_add_u32 m0, s57, 32768\n"
    "s_add_u32 s65, s59, 32768\n"
    "buffer_load_dwordx4 v236, s[40:43], s65 offen nt lds\n"
    "buffer_load_dwordx4 v237, s[40:43], s65 offen offset:1024 nt lds\n"
    "buffer_load_dwordx4 v238, s[40:43], s65 offen offset:2048 nt lds\n"
    "buffer_load_dwordx4 v239, s[40:43], s65 offen offset:3072 nt lds\n"
    "s_add_u32 m0, s57, 98304\n"
    "s_add_u32 s65, s59, 32768\n"
    "buffer_load_dwordx4 v240, s[44:47], s65 offen nt lds\n"
    "buffer_load_dwordx4 v241, s[44:47], s65 offen offset:1024 nt lds\n"
    "buffer_load_dwordx4 v240, s[44:47], s65 offen offset:2048 nt lds\n"
    "buffer_load_dwordx4 v241, s[44:47], s65 offen offset:3072 nt lds\n"
    "v_exp_f32 v176, v176\n"
    "v_exp_f32 v177, v177\n"
    "v_exp_f32 v178, v178\n"
    "v_exp_f32 v179, v179\n"
    "v_exp_f32 v180, v180\n"
    "v_exp_f32 v181, v181\n"
    "v_exp_f32 v182, v182\n"
    "v_exp_f32 v183, v183\n"
    "v_exp_f32 v184, v184\n"
    "v_cvt_pk_bf16_f32 v194, v176, v177\n"
    "v_cvt_pk_bf16_f32 v195, v178, v179\n"
    "v_cvt_pk_bf16_f32 v198, v180, v181\n"
    "v_cvt_pk_bf16_f32 v199, v182, v183\n"
    "s_add_u32 s56, s59, 49152\n"
    "s_lshl_b32 s63, %[w], 14\n"
    "s_add_u32 s63, s63, 98304\n"
    "s_cmp_eq_u32 %[w], 1\n"
    "s_cselect_b32 s63, 98304, s63\n"
    "s_cmp_eq_u32 %[w], 0\n"
    "s_cselect_b32 s63, 0, s63\n"
    "s_add_u32 s63, s63, %[lds]\n"
    "v_and_b32 v208, 15, v242\n"
    "v_lshrrev_b32 v209, 5, v242\n"
    "v_and_b32 v210, 7, v242\n"
    "v_xor_b32 v209, v209, v210\n"
    "v_lshlrev_b32 v209, 4, v209\n"
    "v_lshl_add_u32 v209, v208, 8, v209\n"
    "v_bfe_u32 v210, v242, 4, 1\n"
    "v_lshl_add_u32 v209, v210, 3, v209\n"
    "v_xor_b32 v244, 0, v209\n"
    "v_add_u32 v244, s63, v244\n"
    "v_xor_b32 v245, 32, v209\n"
    "v_add_u32 v245, s63, v245\n"
    "v_xor_b32 v246, 64, v209\n"
    "v_add_u32 v246, s63, v246\n"
    "v_xor_b32 v247, 96, v209\n"
    "v_add_u32 v247, s63, v247\n"
    "v_lshrrev_b32 v210, 4, v242\n"
    "v_xor_b32 v208, v208, v210\n"
    "v_lshlrev_b32 v208, 4, v208\n"
    "v_lshl_add_u32 v208, v210, 8, v208\n"
    "v_add_u32 v248, s63, v208\n"
    "v_xor_b32 v249, 64, v208\n"
    "v_add_u32 v249, 1024, v249\n"
    "v_add_u32 v249, s63, v249\n"
    "v_lshlrev_b32 v250, 4, v242\n"
    "s_lshl_b32 s59, %[w], 14\n"
    "s_memtime s[82:83]\n"
    "s_memrealtime s[84:85]\n"
    "s_waitcnt vmcnt(16)\n"
    "s_barrier\n"
    "ds_read_b64_tr_b16 a[96:97], v228\n"
    "ds_read_b64_tr_b16 a[98:99], v228 offset:4096\n"
    "ds_read_b64_tr_b16 a[100:101], v229\n"
    "ds_read_b64_tr_b16 a[102:103], v229 offset:4096\n"
    "ds_read_b64_tr_b16 a[104:105], v230\n"
    "ds_read_b64_tr_b16 a[106:107], v230 offset:4096\n"
    "ds_read_b64_tr_b16 a[108:109], v231\n"
    "ds_read_b64_tr_b16 a[110:111], v231 offset:4096\n"
    "s_waitcnt lgkmcnt(8)\n"
    "s_mov_b32 s58, 8\n"
    "s_waitcnt lgkmcnt(0)\n"
    "s_sub_u32 s86, s82, s86\n"
    "s_sub_u32 s87, s84, s87\n"
    "s_lshl_b32 s86, s86, 1\n"
    "s_mul_i32 s87, s87, 38\n"
    "s_cmp_gt_u32 s86, s87\n"
    "s_cselect_b32 s88, 1, 0\n"
    "s_mov_b32 s86, s82\n"
    "s_mov_b32 s87, s84\n"
    "s_mov_b32 s78, s84\n"
    "s_mov_b32 s80, 102\n"
    "s_cmp_eq_u32 s88, 0\n"
    "s_cselect_b32 s80, 70, s80\n"
    "s_cbranch_scc1 Lfast_%=\n"
    "Lslow_%=:\n"
    "v_mfma_f32_16x16x32_bf16 v[160:163], a[64:67], a[0:3], v[144:147]\n"
    "v_exp_f32 v185, v185\n"
    "v_mfma_f32_16x16x32_bf16 v[160:163], a[68:71], a[4:7], v[160:163]\n"
    "v_cvt_pk_bf16_f32 v202, v184, v185\n"
    "s_nop 0\n"
    "v_mfma_f32_16x16x32_bf16 v[160:163], a[72:75], a[8:11], v[160:163]\n"
    "v_exp_f32 v186, v186\n"
    "v_mfma_f32_16x16x32_bf16 v[160:163], a[76:79], a[12:15], v[160:163]\n"
    "ds_read_b128 a[80:83], v224 offset:12288\n"
    "ds_read_b128 a[84:87], v225 offset:12288\n"
    "s_nop 0\n"
    "v_mfma_f32_16x16x32_bf16 v[164:167], a[64:67], a[16:19], v[148:151]\n"
    "v_exp_f32 v187, v187\n"
    "v_mfma_f32_16x16x32_bf16 v[164:167], a[68:71], a[20:23], v[164:167]\n"
    "v_cvt_pk_bf16_f32 v203, v186, v187\n"
    "s_nop 0\n"
    "v_mfma_f32_16x16x32_bf16 v[164:167], a[72:75], a[24:27], v[164:167]\n"
    "v_exp_f32 v188, v188\n"
    "v_mfma_f32_16x16x32_bf16 v[164:167], a[76:79], a[28:31], v[164:167]\n"
    "ds_read_b128 a[88:91], v226 offset:12288\n"
    "ds_read_b128 a[92:95], v227 offset:12288\n"
    "s_nop 0\n"
    "v_mfma_f32_16x16x32_bf16 v[168:171], a[64:67], a[32:35], v[152:155]\n"
    "v_exp_f32 v189, v189\n"
    "s_memrealtime s[76:77]\n"
    "v_mfma_f32_16x16x32_bf16 v[168:171], a[68:71], a[36:39], v[168:171]\n"
    "v_cvt_pk_bf16_f32 v206, v188, v189\n"
    "s_add_u32 m0, s57, 49152\n"
    "s_nop 0\n"
    "v_mfma_f32_16x16x32_bf16 v[168:171], a[72:75], a[40:43], v[168:171]\n"
    "v_exp_f32 v190, v190\n"
    "v_mfma_f32_16x16x32_bf16 v[168:171], a[76:79], a[44:47], v[168:171]\n"
    "buffer_load_dwordx4 v236, s[40:43], s56 offen nt lds\n"
    "s_nop 0\n"
    "v_mfma_f32_16x16x32_bf16 v[172:175], a[64:67], a[48:51], v[156:159]\n"
    "v_exp_f32 v191, v191\n"
    "v_mfma_f32_16x16x32_bf16 v[172:175], a[68:71], a[52:55], v[172:175]\n"
    "v_cvt_pk_bf16_f32 v207, v190, v191\n"
    "s_nop 0\n"
    "v_mfma_f32_16x16x32_bf16 v[172:175], a[72:75], a[56:59], v[172:175]\n"
    "s_waitcnt lgkmcnt(0)\n"
    "s_sub_u32 s79, s78, s76\n"
    "v_mfma_f32_16x16x32_bf16 v[172:175], a[76:79], a[60:63], v[172:175]\n"
    "s_cmp_gt_i32 s79, 0\n"
    "s_cbranch_scc1 Lpw2_%=\n"
    "Lpw2_%=b:\n"
    "s_nop 0\n"
    "v_mfma_f32_16x16x32_bf16 v[0:3], a[96:99], v[192:195], v[0:3]\n"
    "s_max_i32 s79, s79, -12\n"
    "s_add_u32 s78, s76, s79\n"
    "v_mfma_f32_16x16x32_bf16 v[4:7], a[100:103], v[192:195], v[4:7]\n"
    "buffer_load_dwordx4 v237, s[40:43], s56 offen offset:1024 nt lds\n"
    "s_nop 0\n"
    "v_mfma_f32_16x16x32_bf16 v[8:11], a[104:107], v[192:195], v[8:11]\n"
    "v_exp_f32 v160, v160\n"
    "v_mfma_f32_16x16x32_bf16 v[12:15], a[108:111], v[192:195], v[12:15]\n"
    "ds_read_b64_tr_b16 a[112:113], v232\n"
    "ds_read_b64_tr_b16 a[114:115], v232 offset:4096\n"
    "s_nop 0\n"
    "v_mfma_f32_16x16x32_bf16 v[32:35], a[96:99], v[196:199], v[32:35]\n"
    "v_exp_f32 v161, v161\n"
    "v_mfma_f32_16x16x32_bf16 v[36:39], a[100:103], v[196:199], v[36:39]\n"
    "v_cvt_pk_bf16_f32 v208, v160, v161\n"
    "s_nop 0\n"
    "v_mfma_f32_16x16x32_bf16 v[40:43], a[104:107], v[196:199], v[40:43]\n"
    "v_exp_f32 v162, v162\n"
    "v_mfma_f32_16x16x32_bf16 v[44:47], a[108:111], v[196:199], v[44:47]\n"
    "ds_read_b64_tr_b16 a[116:117], v233\n"
    "ds_read_b64_tr_b16 a[118:119], v233 offset:4096\n"
    "s_nop 0\n"
    "v_mfma_f32_16x16x32_bf16 v[64:67], a[96:99], v[200:203], v[64:67]\n"
    "v_exp_f32 v163, v163\n"
    "v_mfma_f32_16x16x32_bf16 v[68:71], a[100:103], v[200:203], v[68:71]\n"
    "v_cvt_pk_bf16_f32 v209, v162, v163\n"
    "s_lshr_b32 s79, s80, 1\n"
    "s_add_u32 s78, s78, s79\n"
    "s_nop 0\n"
    "v_mfma_f32_16x16x32_bf16 v[72:75], a[104:107], v[200:203], v[72:75]\n"
    "v_exp_f32 v164, v164\n"
    "v_mfma_f32_16x16x32_bf16 v[76:79], a[108:111], v[200:203], v[76:79]\n"
    "ds_read_b64_tr_b16 a[120:121], v234\n"
    "ds_read_b64_tr_b16 a[122:123], v234 offset:4096\n"
    "s_nop 0\n"
    "v_mfma_f32_16x16x32_bf16 v[96:99], a[96:99], v[204:207], v[96:99]\n"
    "v_exp_f32 v165, v165\n"
    "v_mfma_f32_16x16x32_bf16 v[100:103], a[100:103], v[204:207], v[100:103]\n"
    "v_cvt_pk_bf16_f32 v212, v164, v165\n"
    "s_nop 0\n"
    "v_mfma_f32_16x16x32_bf16 v[104:107], a[104:107], v[204:207], v[104:107]\n"
    "v_exp_f32 v166, v166\n"
    "v_mfma_f32_16x16x32_bf16 v[108:111], a[108:111], v[204:207], v[108:111]\n"
    "ds_read_b64_tr_b16 a[124:125], v235\n"
    "ds_read_b64_tr_b16 a[126:127], v235 offset:4096\n"
    "s_nop 0\n"
    "v_mfma_f32_16x16x32_bf16 v[176:179], a[80:83], a[0:3], v[144:147]\n"
    "v_exp_f32 v167, v167\n"
    "v_mfma_f32_16x16x32_bf16 v[176:179], a[84:87], a[4:7], v[176:179]\n"
    "v_cvt_pk_bf16_f32 v213, v166, v167\n"
    "s_cmp_eq_u32 s58, 8\n"
    "s_cbranch_scc1 Lb0_1_%=\n"
    "Lb0_1_%=b:\n"
    "s_sub_u32 s80, s80, 4\n"
    "s_nop 0\n"
    "v_mfma_f32_16x16x32_bf16 v[176:179], a[88:91], a[8:11], v[176:179]\n"
    "v_exp_f32 v168, v168\n"
    "v_mfma_f32_16x16x32_bf16 v[176:179], a[92:95], a[12:15], v[176:179]\n"
    "ds_read_b128 a[64:67], v224 offset:16384\n"
    "ds_read_b128 a[68:71], v225 offset:16384\n"
    "s_nop 0\n"
    "v_mfma_f32_16x16x32_bf16 v[180:183], a[80:83], a[16:19], v[148:151]\n"
    "v_exp_f32 v169, v169\n"
    "v_mfma_f32_16x16x32_bf16 v[180:183], a[84:87], a[20:23], v[180:183]\n"
    "v_cvt_pk_bf16_f32 v216, v168, v169\n"
    "s_max_i32 s80, s80, 70\n"
    "s_nop 0\n"
    "v_mfma_f32_16x16x32_bf16 v[180:183], a[88:91], a[24:27], v[180:183]\n"
    "v_exp_f32 v170, v170\n"
    "v_mfma_f32_16x16x32_bf16 v[180:183], a[92:95], a[28:31], v[180:183]\n"
    "ds_read_b128 a[72:75], v226 offset:16384\n"
    "ds_read_b128 a[76:79], v227 offset:16384\n"
    "s_nop 0\n"
    "v_mfma_f32_16x16x32_bf16 v[184:187], a[80:83], a[32:35], v[152:155]\n"
    "v_exp_f32 v171, v171\n"
    "v_mfma_f32_16x16x32_bf16 v[184:187], a[84:87], a[36:39], v[184:187]\n"
    "v_cvt_pk_bf16_f32 v217, v170, v171\n"
    "s_memrealtime s[76:77]\n"
    "s_nop 0\n"
    "v_mfma_f32_16x16x32_bf16 v[184:187], a[88:91], a[40:43], v[184:187]\n"
    "v_exp_f32 v172, v172\n"
    "v_mfma_f32_16x16x32_bf16 v[184:187], a[92:95], a[44:47], v[184:187]\n"
    "buffer_load_dwordx4 v238, s[40:43], s56 offen offset:2048 nt lds\n"
    "s_nop 0\n"
    "v_mfma_f32_16x16x32_bf16 v[188:191], a[80:83], a[48:51], v[156:159]\n"
    "v_exp_f32 v173, v173\n"
    "v_mfma_f32_16x16x32_bf16 v[188:191], a[84:87], a[52:55], v[188:191]\n"
    "v_cvt_pk_bf16_f32 v220, v172, v173\n"
    "s_nop 0\n"
    "v_mfma_f32_16x16x32_bf16 v[188:191], a[88:91], a[56:59], v[188:191]\n"
    "v_exp_f32 v174, v174\n"
    "v_mfma_f32_16x16x32_bf16 v[188:191], a[92:95], a[60:63], v[188:191]\n"
    "s_waitcnt lgkmcnt(0)\n"
    "s_sub_u32 s79, s78, s76\n"
    "s_cmp_gt_i32 s79, 0\n"
    "s_cbranch_scc1 Lpw3_%=\n"
    "Lpw3_%=b:\n"
    "s_max_i32 s79, s79, -12\n"
    "s_add_u32 s78, s76, s79\n"
    "s_lshr_b32 s79, s80, 1\n"
    "s_add_u32 s78, s78, s79\n"
    "s_nop 0\n"
    "v_mfma_f32_16x16x32_bf16 v[16:19], a[112:115], v[192:195], v[16:19]\n"
    "v_exp_f32 v175, v175\n"
    "v_mfma_f32_16x16x32_bf16 v[20:23], a[116:119], v[192:195], v[20:23]\n"
    "buffer_load_dwordx4 v239, s[40:43], s56 offen offset:3072 nt lds\n"
    "v_cvt_pk_bf16_f32 v221, v174, v175\n"
    "s_nop 0\n"
    "v_mfma_f32_16x16x32_bf16 v[24:27], a[120:123], v[192:195], v[24:27]\n"
    "v_exp_f32 v176, v176\n"
    "v_mfma_f32_16x16x32_bf16 v[28:31], a[124:127], v[192:195], v[28:31]\n"
    "s_nop 0\n"
    "v_mfma_f32_16x16x32_bf16 v[48:51], a[112:115], v[196:199], v[48:51]\n"
    "v_exp_f32 v177, v177\n"
    "v_mfma_f32_16x16x32_bf16 v[52:55], a[116:119], v[196:199], v[52:55]\n"
    "v_cvt_pk_bf16_f32 v210, v176, v177\n"
    "s_nop 0\n"
    "v_mfma_f32_16x16x32_bf16 v[56:59], a[120:123], v[196:199], v[56:59]\n"
    "v_exp_f32 v178, v178\n"
    "v_mfma_f32_16x16x32_bf16 v[60:63], a[124:127], v[196:199], v[60:63]\n"
    "ds_read_b64_tr_b16 a[96:97], v228 offset:8192\n"
    "ds_read_b64_tr_b16 a[98:99], v228 offset:12288\n"
    "s_nop 0\n"
    "v_mfma_f32_16x16x32_bf16 v[80:83], a[112:115], v[200:203], v[80:83]\n"
    "v_exp_f32 v179, v179\n"
    "v_mfma_f32_16x16x32_bf16 v[84:87], a[116:119], v[200:203], v[84:87]\n"
    "v_cvt_pk_bf16_f32 v211, v178, v179\n"
    "s_nop 0\n"
    "v_mfma_f32_16x16x32_bf16 v[88:91], a[120:123], v[200:203], v[88:91]\n"
    "v_exp_f32 v180, v180\n"
    "v_mfma_f32_16x16x32_bf16 v[92:95], a[124:127], v[200:203], v[92:95]\n"
    "ds_read_b64_tr_b16 a[100:101], v229 offset:8192\n"
    "ds_read_b64_tr_b16 a[102:103], v229 offset:12288\n"
    "s_nop 0\n"
    "v_mfma_f32_16x16x32_bf16 v[112:115], a[112:115], v[204:207], v[112:115]\n"
    "v_exp_f32 v181, v181\n"
    "v_mfma_f32_16x16x32_bf16 v[116:119], a[116:119], v[204:207], v[116:119]\n"
    "v_cvt_pk_bf16_f32 v214, v180, v181\n"
    "s_nop 0\n"
    "v_mfma_f32_16x16x32_bf16 v[120:123], a[120:123], v[204:207], v[120:123]\n"
    "v_exp_f32 v182, v182\n"
    "v_mfma_f32_16x16x32_bf16 v[124:127], a[124:127], v[204:207], v[124:127]\n"
    "ds_read_b64_tr_b16 a[104:105], v230 offset:8192\n"
    "ds_read_b64_tr_b16 a[106:107], v230 offset:12288\n"
    "s_nop 0\n"
    "v_mfma_f32_16x16x32_bf16 v[128:131], a[128:131], v[192:195], v[128:131]\n"
    "v_exp_f32 v183, v183\n"
    "v_mfma_f32_16x16x32_bf16 v[132:135], a[128:131], v[196:199], v[132:135]\n"
    "v_cvt_pk_bf16_f32 v215, v182, v183\n"
    "s_nop 0\n"
    "v_mfma_f32_16x16x32_bf16 v[136:139], a[128:131], v[200:203], v[136:139]\n"
    "v_exp_f32 v184, v184\n"
    "v_mfma_f32_16x16x32_bf16 v[140:143], a[128:131], v[204:207], v[140:143]\n"
    "ds_read_b64_tr_b16 a[108:109], v231 offset:8192\n"
    "ds_read_b64_tr_b16 a[110:111], v231 offset:12288\n"
    "s_nop 0\n"
    "v_mfma_f32_16x16x32_bf16 v[160:163], a[64:67], a[0:3], v[144:147]\n"
    "v_exp_f32 v185, v185\n"
    "v_mfma_f32_16x16x32_bf16 v[160:163], a[68:71], a[4:7], v[160:163]\n"
    "v_cvt_pk_bf16_f32 v218, v184, v185\n"
    "s_nop 0\n"
    "v_mfma_f32_16x16x32_bf16 v[160:163], a[72:75], a[8:11], v[160:163]\n"
    "v_exp_f32 v186, v186\n"
    "v_mfma_f32_16x16x32_bf16 v[160:163], a[76:79], a[12:15], v[160:163]\n"
    "ds_read_b128 a[80:83], v224 offset:20480\n"
    "ds_read_b128 a[84:87], v225 offset:20480\n"
    "s_nop 0\n"
    "v_mfma_f32_16x16x32_bf16 v[164:167], a[64:67], a[16:19], v[148:151]\n"
    "v_exp_f32 v187, v187\n"
    "v_mfma_f32_16x16x32_bf16 v[164:167], a[68:71], a[20:23], v[164:167]\n"
    "v_cvt_pk_bf16_f32 v219, v186, v187\n"
    "s_nop 0\n"
    "v_mfma_f32_16x16x32_bf16 v[164:167], a[72:75], a[24:27], v[164:167]\n"
    "v_exp_f32 v188, v188\n"
    "v_mfma_f32_16x16x32_bf16 v[164:167], a[76:79], a[28:31], v[164:167]\n"
    "ds_read_b128 a[88:91], v226 offset:20480\n"
    "ds_read_b128 a[92:95], v227 offset:20480\n"
    "s_nop 0\n"
    "v_mfma_f32_16x16x32_bf16 v[168:171], a[64:67], a[32:35], v[152:155]\n"
    "v_exp_f32 v189, v189\n"
    "s_memrealtime s[76:77]\n"
    "v_mfma_f32_16x16x32_bf16 v[168:171], a[68:71], a[36:39], v[168:171]\n"
    "v_cvt_pk_bf16_f32 v222, v188, v189\n"
    "s_add_u32 m0, s57, 114688\n"
    "s_nop 0\n"
    "v_mfma_f32_16x16x32_bf16 v[168:171], a[72:75], a[40:43], v[168:171]\n"
    "v_exp_f32 v190, v190\n"
    "v_mfma_f32_16x16x32_bf16 v[168:171], a[76:79], a[44:47], v[168:171]\n"
    "buffer_load_dwordx4 v240, s[44:47], s56 offen nt lds\n"
    "s_nop 0\n"
    "v_mfma_f32_16x16x32_bf16 v[172:175], a[64:67], a[48:51], v[156:159]\n"
    "v_exp_f32 v191, v191\n"
    "v_mfma_f32_16x16x32_bf16 v[172:175], a[68:71], a[52:55], v[172:175]\n"
    "v_cvt_pk_bf16_f32 v223, v190, v191\n"
    "s_nop 0\n"
    "v_mfma_f32_16x16x32_bf16 v[172:175], a[72:75], a[56:59], v[172:175]\n"
    "s_waitcnt lgkmcnt(0)\n"
    "s_sub_u32 s79, s78, s76\n"
    "v_mfma_f32_16x16x32_bf16 v[172:175], a[76:79], a[60:63], v[172:175]\n"
    "s_cmp_gt_i32 s79, 0\n"
    "s_cbranch_scc1 Lpw4_%=\n"
    "Lpw4_%=b:\n"
    "s_nop 0\n"
    "v_mfma_f32_16x16x32_bf16 v[0:3], a[96:99], v[208:211], v[0:3]\n"
    "s_max_i32 s79, s79, -12\n"
    "s_add_u32 s78, s76, s79\n"
    "v_mfma_f32_16x16x32_bf16 v[4:7], a[100:103], v[208:211], v[4:7]\n"
    "buffer_load_dwordx4 v241, s[44:47], s56 offen offset:1024 nt lds\n"
    "s_nop 0\n"
    "v_mfma_f32_16x16x32_bf16 v[8:11], a[104:107], v[208:211], v[8:11]\n"
    "v_exp_f32 v160, v160\n"
    "v_mfma_f32_16x16x32_bf16 v[12:15], a[108:111], v[208:211], v[12:15]\n"
    "ds_read_b64_tr_b16 a[112:113], v232 offset:8192\n"
    "ds_read_b64_tr_b16 a[114:115], v232 offset:12288\n"
    "s_nop 0\n"
    "v_mfma_f32_16x16x32_bf16 v[32:35], a[96:99], v[212:215], v[32:35]\n"
    "v_exp_f32 v161, v161\n"
    "v_mfma_f32_16x16x32_bf16 v[36:39], a[100:103], v[212:215], v[36:39]\n"
    "v_cvt_pk_bf16_f32 v192, v160, v161\n"
    "s_nop 0\n"
    "v_mfma_f32_16x16x32_bf16 v[40:43], a[104:107], v[212:215], v[40:43]\n"
    "v_exp_f32 v162, v162\n"
    "v_mfma_f32_16x16x32_bf16 v[44:47], a[108:111], v[212:215], v[44:47]\n"
    "ds_read_b64_tr_b16 a[116:117], v233 offset:8192\n"
    "ds_read_b64_tr_b16 a[118:119], v233 offset:12288\n"
    "s_nop 0\n"
    "v_mfma_f32_16x16x32_bf16 v[64:67], a[96:99], v[216:219], v[64:67]\n"
    "v_exp_f32 v163, v163\n"
    "v_mfma_f32_16x16x32_bf16 v[68:71], a[100:103], v[216:219], v[68:71]\n"
    "v_cvt_pk_bf16_f32 v193, v162, v163\n"
    "s_lshr_b32 s79, s80, 1\n"
    "s_add_u32 s78, s78, s79\n"
    "s_nop 0\n"
    "v_mfma_f32_16x16x32_bf16 v[72:75], a[104:107], v[216:219], v[72:75]\n"
    "v_exp_f32 v164, v164\n"
    "v_mfma_f32_16x16x32_bf16 v[76:79], a[108:111], v[216:219], v[76:79]\n"
    "ds_read_b64_tr_b16 a[120:121], v234 offset:8192\n"
    "ds_read_b64_tr_b16 a[122:123], v234 offset:12288\n"
    "s_nop 0\n"
    "v_mfma_f32_16x16x32_bf16 v[96:99], a[96:99], v[220:223], v[96:99]\n"
    "v_exp_f32 v165, v165\n"
    "v_mfma_f32_16x16x32_bf16 v[100:103], a[100:103], v[220:223], v[100:103]\n"
    "v_cvt_pk_bf16_f32 v196, v164, v165\n"
    "s_nop 0\n"
    "v_mfma_f32_16x16x32_bf16 v[104:107], a[104:107], v[220:223], v[104:107]\n"
    "v_exp_f32 v166, v166\n"
    "v_mfma_f32_16x16x32_bf16 v[108:111], a[108:111], v[220:223], v[108:111]\n"
    "ds_read_b64_tr_b16 a[124:125], v235 offset:8192\n"
    "ds_read_b64_tr_b16 a[126:127], v235 offset:12288\n"
    "s_nop 0\n"
    "v_mfma_f32_16x16x32_bf16 v[176:179], a[80:83], a[0:3], v[144:147]\n"
    "v_exp_f32 v167, v167\n"
    "v_mfma_f32_16x16x32_bf16 v[176:179], a[84:87], a[4:7], v[176:179]\n"
    "v_cvt_pk_bf16_f32 v197, v166, v167\n"
    "s_sub_u32 s80, s80, 4\n"
    "s_nop 0\n"
    "v_mfma_f32_16x16x32_bf16 v[176:179], a[88:91], a[8:11], v[176:179]\n"
    "v_exp_f32 v168, v168\n"
    "v_mfma_f32_16x16x32_bf16 v[176:179], a[92:95], a[12:15], v[176:179]\n"
    "ds_read_b128 a[64:67], v224 offset:24576\n"
    "ds_read_b128 a[68:71], v225 offset:24576\n"
    "s_nop 0\n"
    "v_mfma_f32_16x16x32_bf16 v[180:183], a[80:83], a[16:19], v[148:151]\n"
    "v_exp_f32 v169, v169\n"
    "v_mfma_f32_16x16x32_bf16 v[180:183], a[84:87], a[20:23], v[180:183]\n"
    "v_cvt_pk_bf16_f32 v200, v168, v169\n"
    "s_max_i32 s80, s80, 70\n"
    "s_nop 0\n"
    "v_mfma_f32_16x16x32_bf16 v[180:183], a[88:91], a[24:27], v[180:183]\n"
    "v_exp_f32 v170, v170\n"
    "v_mfma_f32_16x16x32_bf16 v[180:183], a[92:95], a[28:31], v[180:183]\n"
    "ds_read_b128 a[72:75], v226 offset:24576\n"
    "ds_read_b128 a[76:79], v227 offset:24576\n"
    "s_nop 0\n"
    "v_mfma_f32_16x16x32_bf16 v[184:187], a[80:83], a[32:35], v[152:155]\n"
    "v_exp_f32 v171, v171\n"
    "v_mfma_f32_16x16x32_bf16 v[184:187], a[84:87], a[36:39], v[184:187]\n"
    "v_cvt_pk_bf16_f32 v201, v170, v171\n"
    "s_memrealtime s[76:77]\n"
    "s_nop 0\n"
    "v_mfma_f32_16x16x32_bf16 v[184:187], a[88:91], a[40:43], v[184:187]\n"
    "v_exp_f32 v172, v172\n"
    "v_mfma_f32_16x16x32_bf16 v[184:187], a[92:95], a[44:47], v[184:187]\n"
    "buffer_load_dwordx4 v240, s[44:47], s56 offen offset:2048 nt lds\n"
    "s_nop 0\n"
    "v_mfma_f32_16x16x32_bf16 v[188:191], a[80:83], a[48:51], v[156:159]\n"
    "v_exp_f32 v173, v173\n"
    "v_mfma_f32_16x16x32_bf16 v[188:191], a[84:87], a[52:55], v[188:191]\n"
    "v_cvt_pk_bf16_f32 v204, v172, v173\n"
    "s_nop 0\n"
    "v_mfma_f32_16x16x32_bf16 v[188:191], a[88:91], a[56:59], v[188:191]\n"
    "v_exp_f32 v174, v174\n"
    "v_mfma_f32_16x16x32_bf16 v[188:191], a[92:95], a[60:63], v[188:191]\n"
    "s_waitcnt lgkmcnt(0)\n"
    "s_sub_u32 s79, s78, s76\n"
    "s_cmp_gt_i32 s79, 0\n"
    "s_cbranch_scc1 Lpw5_%=\n"
    "Lpw5_%=b:\n"
    "s_max_i32 s79, s79, -12\n"
    "s_add_u32 s78, s76, s79\n"
    "s_lshr_b32 s79, s80, 1\n"
    "s_add_u32 s78, s78, s79\n"
    "s_nop 0\n"
    "v_mfma_f32_16x16x32_bf16 v[16:19], a[112:115], v[208:211], v[16:19]\n"
    "v_exp_f32 v175, v175\n"
    "v_mfma_f32_16x16x32_bf16 v[20:23], a[116:119], v[208:211], v[20:23]\n"
    "buffer_load_dwordx4 v241, s[44:47], s56 offen offset:3072 nt lds\n"
    "v_cvt_pk_bf16_f32 v205, v174, v175\n"
    "s_add_u32 s56, s56, 16384\n"
    "s_nop 0\n"
    "v_mfma_f32_16x16x32_bf16 v[24:27], a[120:123], v[208:211], v[24:27]\n"
    "v_exp_f32 v176, v176\n"
    "v_mfma_f32_16x16x32_bf16 v[28:31], a[124:127], v[208:211], v[28:31]\n"
    "s_waitcnt vmcnt(12)\n"
    "s_barrier\n"
    "s_nop 0\n"
    "v_mfma_f32_16x16x32_bf16 v[48:51], a[112:115], v[212:215], v[48:51]\n"
    "v_exp_f32 v177, v177\n"
    "v_mfma_f32_16x16x32_bf16 v[52:55], a[116:119], v[212:215], v[52:55]\n"
    "v_cvt_pk_bf16_f32 v194, v176, v177\n"
    "s_nop 0\n"
    "v_mfma_f32_16x16x32_bf16 v[56:59], a[120:123], v[212:215], v[56:59]\n"
    "v_exp_f32 v178, v178\n"
    "v_mfma_f32_16x16x32_bf16 v[60:63], a[124:127], v[212:215], v[60:63]\n"
    "ds_read_b64_tr_b16 a[96:97], v228 offset:16384\n"
    "ds_read_b64_tr_b16 a[98:99], v228 offset:20480\n"
    "s_nop 0\n"
    "v_mfma_f32_16x16x32_bf16 v[80:83], a[112:115], v[216:219], v[80:83]\n"
    "v_exp_f32 v179, v179\n"
    "v_mfma_f32_16x16x32_bf16 v[84:87], a[116:119], v[216:219], v[84:87]\n"
    "v_cvt_pk_bf16_f32 v195, v178, v179\n"
    "s_nop 0\n"
    "v_mfma_f32_16x16x32_bf16 v[88:91], a[120:123], v[216:219], v[88:91]\n"
    "v_exp_f32 v180, v180\n"
    "v_mfma_f32_16x16x32_bf16 v[92:95], a[124:127], v[216:219], v[92:95]\n"
    "ds_read_b64_tr_b16 a[100:101], v229 offset:16384\n"
    "ds_read_b64_tr_b16 a[102:103], v229 offset:20480\n"
    "s_nop 0\n"
    "v_mfma_f32_16x16x32_bf16 v[112:115], a[112:115], v[220:223], v[112:115]\n"
    "v_exp_f32 v181, v181\n"
    "v_mfma_f32_16x16x32_bf16 v[116:119], a[116:119], v[220:223], v[116:119]\n"
    "v_cvt_pk_bf16_f32 v198, v180, v181\n"
    "s_nop 0\n"
    "v_mfma_f32_16x16x32_bf16 v[120:123], a[120:123], v[220:223], v[120:123]\n"
    "v_exp_f32 v182, v182\n"
    "v_mfma_f32_16x16x32_bf16 v[124:127], a[124:127], v[220:223], v[124:127]\n"
    "ds_read_b64_tr_b16 a[104:105], v230 offset:16384\n"
    "ds_read_b64_tr_b16 a[106:107], v230 offset:20480\n"
    "s_nop 0\n"
    "v_mfma_f32_16x16x32_bf16 v[128:131], a[128:131], v[208:211], v[128:131]\n"
    "v_exp_f32 v183, v183\n"
    "v_mfma_f32_16x16x32_bf16 v[132:135], a[128:131], v[212:215], v[132:135]\n"
    "v_cvt_pk_bf16_f32 v199, v182, v183\n"
    "s_nop 0\n"
    "v_mfma_f32_16x16x32_bf16 v[136:139], a[128:131], v[216:219], v[136:139]\n"
    "v_exp_f32 v184, v184\n"
    "v_mfma_f32_16x16x32_bf16 v[140:143], a[128:131], v[220:223], v[140:143]\n"
    "ds_read_b64_tr_b16 a[108:109], v231 offset:16384\n"
    "ds_read_b64_tr_b16 a[110:111], v231 offset:20480\n"
    "s_nop 0\n"
    "v_mfma_f32_16x16x32_bf16 v[160:163], a[64:67], a[0:3], v[144:147]\n"
    "v_exp_f32 v185, v185\n"
    "v_mfma_f32_16x16x32_bf16 v[160:163], a[68:71], a[4:7], v[160:163]\n"
    "v_cvt_pk_bf16_f32 v202, v184, v185\n"
    "s_nop 0\n"
    "v_mfma_f32_16x16x32_bf16 v[160:163], a[72:75], a[8:11], v[160:163]\n"
    "v_exp_f32 v186, v186\n"
    "v_mfma_f32_16x16x32_bf16 v[160:163], a[76:79], a[12:15], v[160:163]\n"
    "ds_read_b128 a[80:83], v224 offset:28672\n"
    "ds_read_b128 a[84:87], v225 offset:28672\n"
    "s_nop 0\n"
    "v_mfma_f32_16x16x32_bf16 v[164:167], a[64:67], a[16:19], v[148:151]\n"
    "v_exp_f32 v187, v187\n"
    "v_mfma_f32_16x16x32_bf16 v[164:167], a[68:71], a[20:23], v[164:167]\n"
    "v_cvt_pk_bf16_f32 v203, v186, v187\n"
    "s_nop 0\n"
    "v_mfma_f32_16x16x32_bf16 v[164:167], a[72:75], a[24:27], v[164:167]\n"
    "v_exp_f32 v188, v188\n"
    "v_mfma_f32_16x16x32_bf16 v[164:167], a[76:79], a[28:31], v[164:167]\n"
    "ds_read_b128 a[88:91], v226 offset:28672\n"
    "ds_read_b128 a[92:95], v227 offset:28672\n"
    "s_nop 0\n"
    "v_mfma_f32_16x16x32_bf16 v[168:171], a[64:67], a[32:35], v[152:155]\n"
    "v_exp_f32 v189, v189\n"
    "s_memrealtime s[76:77]\n"
    "v_mfma_f32_16x16x32_bf16 v[168:171], a[68:71], a[36:39], v[168:171]\n"
    "v_cvt_pk_bf16_f32 v206, v188, v189\n"
    "s_add_u32 m0, s57, 0\n"
    "s_nop 0\n"
    "v_mfma_f32_16x16x32_bf16 v[168:171], a[72:75], a[40:43], v[168:171]\n"
    "v_exp_f32 v190, v190\n"
    "v_mfma_f32_16x16x32_bf16 v[168:171], a[76:79], a[44:47], v[168:171]\n"
    "buffer_load_dwordx4 v236, s[40:43], s56 offen nt lds\n"
    "s_nop 0\n"
    "v_mfma_f32_16x16x32_bf16 v[172:175], a[64:67], a[48:51], v[156:159]\n"
    "v_exp_f32 v191, v191\n"
    "v_mfma_f32_16x16x32_bf16 v[172:175], a[68:71], a[52:55], v[172:175]\n"
    "v_cvt_pk_bf16_f32 v207, v190, v191\n"
    "s_nop 0\n"
    "v_mfma_f32_16x16x32_bf16 v[172:175], a[72:75], a[56:59], v[172:175]\n"
    "s_waitcnt lgkmcnt(0)\n"
    "s_sub_u32 s79, s78, s76\n"
    "v_mfma_f32_16x16x32_bf16 v[172:175], a[76:79], a[60:63], v[172:175]\n"
    "s_cmp_gt_i32 s79, 0\n"
    "s_cbranch_scc1 Lpw6_%=\n"
    "Lpw6_%=b:\n"
    "s_nop 0\n"
    "v_mfma_f32_16x16x32_bf16 v[0:3], a[96:99], v[192:195], v[0:3]\n"
    "s_max_i32 s79, s79, -12\n"
    "s_add_u32 s78, s76, s79\n"
    "v_mfma_f32_16x16x32_bf16 v[4:7], a[100:103], v[192:195], v[4:7]\n"
    "buffer_load_dwordx4 v237, s[40:43], s56 offen offset:1024 nt lds\n"
    "s_nop 0\n"
    "v_mfma_f32_16x16x32_bf16 v[8:11], a[104:107], v[192:195], v[8:11]\n"
    "v_exp_f32 v160, v160\n"
    "v_mfma_f32_16x16x32_bf16 v[12:15], a[108:111], v[192:195], v[12:15]\n"
    "ds_read_b64_tr_b16 a[112:113], v232 offset:16384\n"
    "ds_read_b64_tr_b16 a[114:115], v232 offset:20480\n"
    "s_nop 0\n"
    "v_mfma_f32_16x16x32_bf16 v[32:35], a[96:99], v[196:199], v[32:35]\n"
    "v_exp_f32 v161, v161\n"
    "v_mfma_f32_16x16x32_bf16 v[36:39], a[100:103], v[196:199], v[36:39]\n"
    "v_cvt_pk_bf16_f32 v208, v160, v161\n"
    "s_nop 0\n"
    "v_mfma_f32_16x16x32_bf16 v[40:43], a[104:107], v[196:199], v[40:43]\n"
    "v_exp_f32 v162, v162\n"
    "v_mfma_f32_16x16x32_bf16 v[44:47], a[108:111], v[196:199], v[44:47]\n"
    "ds_read_b64_tr_b16 a[116:117], v233 offset:16384\n"
    "ds_read_b64_tr_b16 a[118:119], v233 offset:20480\n"
    "s_nop 0\n"
    "v_mfma_f32_16x16x32_bf16 v[64:67], a[96:99], v[200:203], v[64:67]\n"
    "v_exp_f32 v163, v163\n"
    "v_mfma_f32_16x16x32_bf16 v[68:71], a[100:103], v[200:203], v[68:71]\n"
    "v_cvt_pk_bf16_f32 v209, v162, v163\n"
    "s_lshr_b32 s79, s80, 1\n"
    "s_add_u32 s78, s78, s79\n"
    "s_nop 0\n"
    "v_mfma_f32_16x16x32_bf16 v[72:75], a[104:107], v[200:203], v[72:75]\n"
    "v_exp_f32 v164, v164\n"
    "v_mfma_f32_16x16x32_bf16 v[76:79], a[108:111], v[200:203], v[76:79]\n"
    "ds_read_b64_tr_b16 a[120:121], v234 offset:16384\n"
    "ds_read_b64_tr_b16 a[122:123], v234 offset:20480\n"
    "s_nop 0\n"
    "v_mfma_f32_16x16x32_bf16 v[96:99], a[96:99], v[204:207], v[96:99]\n"
    "v_exp_f32 v165, v165\n"
    "v_mfma_f32_16x16x32_bf16 v[100:103], a[100:103], v[204:207], v[100:103]\n"
    "v_cvt_pk_bf16_f32 v212, v164, v165\n"
    "s_nop 0\n"
    "v_mfma_f32_16x16x32_bf16 v[104:107], a[104:107], v[204:207], v[104:107]\n"
    "v_exp_f32 v166, v166\n"
    "v_mfma_f32_16x16x32_bf16 v[108:111], a[108:111], v[204:207], v[108:111]\n"
    "ds_read_b64_tr_b16 a[124:125], v235 offset:16384\n"
    "ds_read_b64_tr_b16 a[126:127], v235 offset:20480\n"
    "s_nop 0\n"
    "v_mfma_f32_16x16x32_bf16 v[176:179], a[80:83], a[0:3], v[144:147]\n"
    "v_exp_f32 v167, v167\n"
    "v_mfma_f32_16x16x32_bf16 v[176:179], a[84:87], a[4:7], v[176:179]\n"
    "v_cvt_pk_bf16_f32 v213, v166, v167\n"
    "s_sub_u32 s80, s80, 4\n"
    "s_nop 0\n"
    "v_mfma_f32_16x16x32_bf16 v[176:179], a[88:91], a[8:11], v[176:179]\n"
    "v_exp_f32 v168, v168\n"
    "v_mfma_f32_16x16x32_bf16 v[176:179], a[92:95], a[12:15], v[176:179]\n"
    "ds_read_b128 a[64:67], v224 offset:32768\n"
    "ds_read_b128 a[68:71], v225 offset:32768\n"
    "s_nop 0\n"
    "v_mfma_f32_16x16x32_bf16 v[180:183], a[80:83], a[16:19], v[148:151]\n"
    "v_exp_f32 v169, v169\n"
    "v_mfma_f32_16x16x32_bf16 v[180:183], a[84:87], a[20:23], v[180:183]\n"
    "v_cvt_pk_bf16_f32 v216, v168, v169\n"
    "s_max_i32 s80, s80, 70\n"
    "s_nop 0\n"
    "v_mfma_f32_16x16x32_bf16 v[180:183], a[88:91], a[24:27], v[180:183]\n"
    "v_exp_f32 v170, v170\n"
    "v_mfma_f32_16x16x32_bf16 v[180:183], a[92:95], a[28:31], v[180:183]\n"
    "ds_read_b128 a[72:75], v226 offset:32768\n"
    "ds_read_b128 a[76:79], v227 offset:32768\n"
    "s_nop 0\n"
    "v_mfma_f32_16x16x32_bf16 v[184:187], a[80:83], a[32:35], v[152:155]\n"
    "v_exp_f32 v171, v171\n"
    "v_mfma_f32_16x16x32_bf16 v[184:187], a[84:87], a[36:39], v[184:187]\n"
    "v_cvt_pk_bf16_f32 v217, v170, v171\n"
    "s_memrealtime s[76:77]\n"
    "s_nop 0\n"
    "v_mfma_f32_16x16x32_bf16 v[184:187], a[88:91], a[40:43], v[184:187]\n"
    "v_exp_f32 v172, v172\n"
    "v_mfma_f32_16x16x32_bf16 v[184:187], a[92:95], a[44:47], v[184:187]\n"
    "buffer_load_dwordx4 v238, s[40:43], s56 offen offset:2048 nt lds\n"
    "s_nop 0\n"
    "v_mfma_f32_16x16x32_bf16 v[188:191], a[80:83], a[48:51], v[156:159]\n"
    "v_exp_f32 v173, v173\n"
    "v_mfma_f32_16x16x32_bf16 v[188:191], a[84:87], a[52:55], v[188:191]\n"
    "v_cvt_pk_bf16_f32 v220, v172, v173\n"
    "s_nop 0\n"
    "v_mfma_f32_16x16x32_bf16 v[188:191], a[88:91], a[56:59], v[188:191]\n"
    "v_exp_f32 v174, v174\n"
    "v_mfma_f32_16x16x32_bf16 v[188:191], a[92:95], a[60:63], v[188:191]\n"
    "s_waitcnt lgkmcnt(0)\n"
    "s_sub_u32 s79, s78, s76\n"
    "s_cmp_gt_i32 s79, 0\n"
    "s_cbranch_scc1 Lpw7_%=\n"
    "Lpw7_%=b:\n"
    "s_max_i32 s79, s79, -12\n"
    "s_add_u32 s78, s76, s79\n"
    "s_lshr_b32 s79, s80, 1\n"
    "s_add_u32 s78, s78, s79\n"
    "s_nop 0\n"
    "v_mfma_f32_16x16x32_bf16 v[16:19], a[112:115], v[192:195], v[16:19]\n"
    "v_exp_f32 v175, v175\n"
    "v_mfma_f32_16x16x32_bf16 v[20:23], a[116:119], v[192:195], v[20:23]\n"
    "buffer_load_dwordx4 v239, s[40:43], s56 offen offset:3072 nt lds\n"
    "v_cvt_pk_bf16_f32 v221, v174, v175\n"
    "s_nop 0\n"
    "v_mfma_f32_16x16x32_bf16 v[24:27], a[120:123], v[192:195], v[24:27]\n"
    "v_exp_f32 v176, v176\n"
    "v_mfma_f32_16x16x32_bf16 v[28:31], a[124:127], v[192:195], v[28:31]\n"
    "s_nop 0\n"
    "v_mfma_f32_16x16x32_bf16 v[48:51], a[112:115], v[196:199], v[48:51]\n"
    "v_exp_f32 v177, v177\n"
    "v_mfma_f32_16x16x32_bf16 v[52:55], a[116:119], v[196:199], v[52:55]\n"
    "v_cvt_pk_bf16_f32 v210, v176, v177\n"
    "s_nop 0\n"
    "v_mfma_f32_16x16x32_bf16 v[56:59], a[120:123], v[196:199], v[56:59]\n"
    "v_exp_f32 v178, v178\n"
    "v_mfma_f32_16x16x32_bf16 v[60:63], a[124:127], v[196:199], v[60:63]\n"
    "ds_read_b64_tr_b16 a[96:97], v228 offset:24576\n"
    "ds_read_b64_tr_b16 a[98:99], v228 offset:28672\n"
    "s_nop 0\n"
    "v_mfma_f32_16x16x32_bf16 v[80:83], a[112:115], v[200:203], v[80:83]\n"
    "v_exp_f32 v179, v179\n"
    "v_mfma_f32_16x16x32_bf16 v[84:87], a[116:119], v[200:203], v[84:87]\n"
    "v_cvt_pk_bf16_f32 v211, v178, v179\n"
    "s_nop 0\n"
    "v_mfma_f32_16x16x32_bf16 v[88:91], a[120:123], v[200:203], v[88:91]\n"
    "v_exp_f32 v180, v180\n"
    "v_mfma_f32_16x16x32_bf16 v[92:95], a[124:127], v[200:203], v[92:95]\n"
    "ds_read_b64_tr_b16 a[100:101], v229 offset:24576\n"
    "ds_read_b64_tr_b16 a[102:103], v229 offset:28672\n"
    "s_nop 0\n"
    "v_mfma_f32_16x16x32_bf16 v[112:115], a[112:115], v[204:207], v[112:115]\n"
    "v_exp_f32 v181, v181\n"
    "v_mfma_f32_16x16x32_bf16 v[116:119], a[116:119], v[204:207], v[116:119]\n"
    "v_cvt_pk_bf16_f32 v214, v180, v181\n"
    "s_nop 0\n"
    "v_mfma_f32_16x16x32_bf16 v[120:123], a[120:123], v[204:207], v[120:123]\n"
    "v_exp_f32 v182, v182\n"
    "v_mfma_f32_16x16x32_bf16 v[124:127], a[124:127], v[204:207], v[124:127]\n"
    "ds_read_b64_tr_b16 a[104:105], v230 offset:24576\n"
    "ds_read_b64_tr_b16 a[106:107], v230 offset:28672\n"
    "s_nop 0\n"
    "v_mfma_f32_16x16x32_bf16 v[128:131], a[128:131], v[192:195], v[128:131]\n"
    "v_exp_f32 v183, v183\n"
    "v_mfma_f32_16x16x32_bf16 v[132:135], a[128:131], v[196:199], v[132:135]\n"
    "v_cvt_pk_bf16_f32 v215, v182, v183\n"
    "s_nop 0\n"
    "v_mfma_f32_16x16x32_bf16 v[136:139], a[128:131], v[200:203], v[136:139]\n"
    "v_exp_f32 v184, v184\n"
    "v_mfma_f32_16x16x32_bf16 v[140:143], a[128:131], v[204:207], v[140:143]\n"
    "ds_read_b64_tr_b16 a[108:109], v231 offset:24576\n"
    "ds_read_b64_tr_b16 a[110:111], v231 offset:28672\n"
    "s_nop 0\n"
    "v_mfma_f32_16x16x32_bf16 v[160:163], a[64:67], a[0:3], v[144:147]\n"
    "v_exp_f32 v185, v185\n"
    "v_mfma_f32_16x16x32_bf16 v[160:163], a[68:71], a[4:7], v[160:163]\n"
    "v_cvt_pk_bf16_f32 v218, v184, v185\n"
    "s_nop 0\n"
    "v_mfma_f32_16x16x32_bf16 v[160:163], a[72:75], a[8:11], v[160:163]\n"
    "v_exp_f32 v186, v186\n"
    "v_mfma_f32_16x16x32_bf16 v[160:163], a[76:79], a[12:15], v[160:163]\n"
    "ds_read_b128 a[80:83], v224 offset:36864\n"
    "ds_read_b128 a[84:87], v225 offset:36864\n"
    "s_nop 0\n"
    "v_mfma_f32_16x16x32_bf16 v[164:167], a[64:67], a[16:19], v[148:151]\n"
    "v_exp_f32 v187, v187\n"
    "v_mfma_f32_16x16x32_bf16 v[164:167], a[68:71], a[20:23], v[164:167]\n"
    "v_cvt_pk_bf16_f32 v219, v186, v187\n"
    "s_nop 0\n"
    "v_mfma_f32_16x16x32_bf16 v[164:167], a[72:75], a[24:27], v[164:167]\n"
    "v_exp_f32 v188, v188\n"
    "v_mfma_f32_16x16x32_bf16 v[164:167], a[76:79], a[28:31], v[164:167]\n"
    "ds_read_b128 a[88:91], v226 offset:36864\n"
    "ds_read_b128 a[92:95], v227 offset:36864\n"
    "s_nop 0\n"
    "v_mfma_f32_16x16x32_bf16 v[168:171], a[64:67], a[32:35], v[152:155]\n"
    "v_exp_f32 v189, v189\n"
    "s_memrealtime s[76:77]\n"
    "v_mfma_f32_16x16x32_bf16 v[168:171], a[68:71], a[36:39], v[168:171]\n"
    "v_cvt_pk_bf16_f32 v222, v188, v189\n"
    "s_add_u32 m0, s57, 65536\n"
    "s_nop 0\n"
    "v_mfma_f32_16x16x32_bf16 v[168:171], a[72:75], a[40:43], v[168:171]\n"
    "v_exp_f32 v190, v190\n"
    "v_mfma_f32_16x16x32_bf16 v[168:171], a[76:79], a[44:47], v[168:171]\n"
    "buffer_load_dwordx4 v240, s[44:47], s56 offen nt lds\n"
    "s_nop 0\n"
    "v_mfma_f32_16x16x32_bf16 v[172:175], a[64:67], a[48:51], v[156:159]\n"
    "v_exp_f32 v191, v191\n"
    "v_mfma_f32_16x16x32_bf16 v[172:175], a[68:71], a[52:55], v[172:175]\n"
    "v_cvt_pk_bf16_f32 v223, v190, v191\n"
    "s_nop 0\n"
    "v_mfma_f32_16x16x32_bf16 v[172:175], a[72:75], a[56:59], v[172:175]\n"
    "s_waitcnt lgkmcnt(0)\n"
    "s_sub_u32 s79, s78, s76\n"
    "v_mfma_f32_16x16x32_bf16 v[172:175], a[76:79], a[60:63], v[172:175]\n"
    "s_cmp_gt_i32 s79, 0\n"
    "s_cbranch_scc1 Lpw8_%=\n"
    "Lpw8_%=b:\n"
    "s_nop 0\n"
    "v_mfma_f32_16x16x32_bf16 v[0:3], a[96:99], v[208:211], v[0:3]\n"
    "s_max_i32 s79, s79, -12\n"
    "s_add_u32 s78, s76, s79\n"
    "v_mfma_f32_16x16x32_bf16 v[4:7], a[100:103], v[208:211], v[4:7]\n"
    "buffer_load_dwordx4 v241, s[44:47], s56 offen offset:1024 nt lds\n"
    "s_nop 0\n"
    "v_mfma_f32_16x16x32_bf16 v[8:11], a[104:107], v[208:211], v[8:11]\n"
    "v_exp_f32 v160, v160\n"
    "v_mfma_f32_16x16x32_bf16 v[12:15], a[108:111], v[208:211], v[12:15]\n"
    "ds_read_b64_tr_b16 a[112:113], v232 offset:24576\n"
    "ds_read_b64_tr_b16 a[114:115], v232 offset:28672\n"
    "s_nop 0\n"
    "v_mfma_f32_16x16x32_bf16 v[32:35], a[96:99], v[212:215], v[32:35]\n"
    "v_exp_f32 v161, v161\n"
    "v_mfma_f32_16x16x32_bf16 v[36:39], a[100:103], v[212:215], v[36:39]\n"
    "v_cvt_pk_bf16_f32 v192, v160, v161\n"
    "s_nop 0\n"
    "v_mfma_f32_16x16x32_bf16 v[40:43], a[104:107], v[212:215], v[40:43]\n"
    "v_exp_f32 v162, v162\n"
    "v_mfma_f32_16x16x32_bf16 v[44:47], a[108:111], v[212:215], v[44:47]\n"
    "ds_read_b64_tr_b16 a[116:117], v233 offset:24576\n"
    "ds_read_b64_tr_b16 a[118:119], v233 offset:28672\n"
    "s_nop 0\n"
    "v_mfma_f32_16x16x32_bf16 v[64:67], a[96:99], v[216:219], v[64:67]\n"
    "v_exp_f32 v163, v163\n"
    "v_mfma_f32_16x16x32_bf16 v[68:71], a[100:103], v[216:219], v[68:71]\n"
    "v_cvt_pk_bf16_f32 v193, v162, v163\n"
    "s_lshr_b32 s79, s80, 1\n"
    "s_add_u32 s78, s78, s79\n"
    "s_nop 0\n"
    "v_mfma_f32_16x16x32_bf16 v[72:75], a[104:107], v[216:219], v[72:75]\n"
    "v_exp_f32 v164, v164\n"
    "v_mfma_f32_16x16x32_bf16 v[76:79], a[108:111], v[216:219], v[76:79]\n"
    "ds_read_b64_tr_b16 a[120:121], v234 offset:24576\n"
    "ds_read_b64_tr_b16 a[122:123], v234 offset:28672\n"
    "s_nop 0\n"
    "v_mfma_f32_16x16x32_bf16 v[96:99], a[96:99], v[220:223], v[96:99]\n"
    "v_exp_f32 v165, v165\n"
    "v_mfma_f32_16x16x32_bf16 v[100:103], a[100:103], v[220:223], v[100:103]\n"
    "v_cvt_pk_bf16_f32 v196, v164, v165\n"
    "s_nop 0\n"
    "v_mfma_f32_16x16x32_bf16 v[104:107], a[104:107], v[220:223], v[104:107]\n"
    "v_exp_f32 v166, v166\n"
    "v_mfma_f32_16x16x32_bf16 v[108:111], a[108:111], v[220:223], v[108:111]\n"
    "ds_read_b64_tr_b16 a[124:125], v235 offset:24576\n"
    "ds_read_b64_tr_b16 a[126:127], v235 offset:28672\n"
    "s_nop 0\n"
    "v_mfma_f32_16x16x32_bf16 v[176:179], a[80:83], a[0:3], v[144:147]\n"
    "v_exp_f32 v167, v167\n"
    "v_mfma_f32_16x16x32_bf16 v[176:179], a[84:87], a[4:7], v[176:179]\n"
    "v_cvt_pk_bf16_f32 v197, v166, v167\n"
    "s_sub_u32 s80, s80, 4\n"
    "s_nop 0\n"
    "v_mfma_f32_16x16x32_bf16 v[176:179], a[88:91], a[8:11], v[176:179]\n"
    "v_exp_f32 v168, v168\n"
    "v_mfma_f32_16x16x32_bf16 v[176:179], a[92:95], a[12:15], v[176:179]\n"
    "ds_read_b128 a[64:67], v224 offset:40960\n"
    "ds_read_b128 a[68:71], v225 offset:40960\n"
    "s_nop 0\n"
    "v_mfma_f32_16x16x32_bf16 v[180:183], a[80:83], a[16:19], v[148:151]\n"
    "v_exp_f32 v169, v169\n"
    "v_mfma_f32_16x16x32_bf16 v[180:183], a[84:87], a[20:23], v[180:183]\n"
    "v_cvt_pk_bf16_f32 v200, v168, v169\n"
    "s_max_i32 s80, s80, 70\n"
    "s_nop 0\n"
    "v_mfma_f32_16x16x32_bf16 v[180:183], a[88:91], a[24:27], v[180:183]\n"
    "v_exp_f32 v170, v170\n"
    "v_mfma_f32_16x16x32_bf16 v[180:183], a[92:95], a[28:31], v[180:183]\n"
    "ds_read_b128 a[72:75], v226 offset:40960\n"
    "ds_read_b128 a[76:79], v227 offset:40960\n"
    "s_nop 0\n"
    "v_mfma_f32_16x16x32_bf16 v[184:187], a[80:83], a[32:35], v[152:155]\n"
    "v_exp_f32 v171, v171\n"
    "v_mfma_f32_16x16x32_bf16 v[184:187], a[84:87], a[36:39], v[184:187]\n"
    "v_cvt_pk_bf16_f32 v201, v170, v171\n"
    "s_memrealtime s[76:77]\n"
    "s_nop 0\n"
    "v_mfma_f32_16x16x32_bf16 v[184:187], a[88:91], a[40:43], v[184:187]\n"
    "v_exp_f32 v172, v172\n"
    "v_mfma_f32_16x16x32_bf16 v[184:187], a[92:95], a[44:47], v[184:187]\n"
    "buffer_load_dwordx4 v240, s[44:47], s56 offen offset:2048 nt lds\n"
    "s_nop 0\n"
    "v_mfma_f32_16x16x32_bf16 v[188:191], a[80:83], a[48:51], v[156:159]\n"
    "v_exp_f32 v173, v173\n"
    "v_mfma_f32_16x16x32_bf16 v[188:191], a[84:87], a[52:55], v[188:191]\n"
    "v_cvt_pk_bf16_f32 v204, v172, v173\n"
    "s_nop 0\n"
    "v_mfma_f32_16x16x32_bf16 v[188:191], a[88:91], a[56:59], v[188:191]\n"
    "v_exp_f32 v174, v174\n"
    "v_mfma_f32_16x16x32_bf16 v[188:191], a[92:95], a[60:63], v[188:191]\n"
    "s_waitcnt lgkmcnt(0)\n"
    "s_sub_u32 s79, s78, s76\n"
    "s_cmp_gt_i32 s79, 0\n"
    "s_cbranch_scc1 Lpw9_%=\n"
    "Lpw9_%=b:\n"
    "s_max_i32 s79, s79, -12\n"
    "s_add_u32 s78, s76, s79\n"
    "s_lshr_b32 s79, s80, 1\n"
    "s_add_u32 s78, s78, s79\n"
    "s_nop 0\n"
    "v_mfma_f32_16x16x32_bf16 v[16:19], a[112:115], v[208:211], v[16:19]\n"
    "v_exp_f32 v175, v175\n"
    "v_mfma_f32_16x16x32_bf16 v[20:23], a[116:119], v[208:211], v[20:23]\n"
    "buffer_load_dwordx4 v241, s[44:47], s56 offen offset:3072 nt lds\n"
    "v_cvt_pk_bf16_f32 v205, v174, v175\n"
    "s_add_u32 s56, s56, 16384\n"
    "s_nop 0\n"
    "v_mfma_f32_16x16x32_bf16 v[24:27], a[120:123], v[208:211], v[24:27]\n"
    "v_exp_f32 v176, v176\n"
    "v_mfma_f32_16x16x32_bf16 v[28:31], a[124:127], v[208:211], v[28:31]\n"
    "s_waitcnt vmcnt(12)\n"
    "s_barrier\n"
    "s_nop 0\n"
    "v_mfma_f32_16x16x32_bf16 v[48:51], a[112:115], v[212:215], v[48:51]\n"
    "v_exp_f32 v177, v177\n"
    "v_mfma_f32_16x16x32_bf16 v[52:55], a[116:119], v[212:215], v[52:55]\n"
    "v_cvt_pk_bf16_f32 v194, v176, v177\n"
    "s_nop 0\n"
    "v_mfma_f32_16x16x32_bf16 v[56:59], a[120:123], v[212:215], v[56:59]\n"
    "v_exp_f32 v178, v178\n"
    "v_mfma_f32_16x16x32_bf16 v[60:63], a[124:127], v[212:215], v[60:63]\n"
    "ds_read_b64_tr_b16 a[96:97], v228 offset:32768\n"
    "ds_read_b64_tr_b16 a[98:99], v228 offset:36864\n"
    "s_nop 0\n"
    "v_mfma_f32_16x16x32_bf16 v[80:83], a[112:115], v[216:219], v[80:83]\n"
    "v_exp_f32 v179, v179\n"
    "v_mfma_f32_16x16x32_bf16 v[84:87], a[116:119], v[216:219], v[84:87]\n"
    "v_cvt_pk_bf16_f32 v195, v178, v179\n"
    "s_nop 0\n"
    "v_mfma_f32_16x16x32_bf16 v[88:91], a[120:123], v[216:219], v[88:91]\n"
    "v_exp_f32 v180, v180\n"
    "v_mfma_f32_16x16x32_bf16 v[92:95], a[124:127], v[216:219], v[92:95]\n"
    "ds_read_b64_tr_b16 a[100:101], v229 offset:32768\n"
    "ds_read_b64_tr_b16 a[102:103], v229 offset:36864\n"
    "s_nop 0\n"
    "v_mfma_f32_16x16x32_bf16 v[112:115], a[112:115], v[220:223], v[112:115]\n"
    "v_exp_f32 v181, v181\n"
    "v_mfma_f32_16x16x32_bf16 v[116:119], a[116:119], v[220:223], v[116:119]\n"
    "v_cvt_pk_bf16_f32 v198, v180, v181\n"
    "s_nop 0\n"
    "v_mfma_f32_16x16x32_bf16 v[120:123], a[120:123], v[220:223], v[120:123]\n"
    "v_exp_f32 v182, v182\n"
    "v_mfma_f32_16x16x32_bf16 v[124:127], a[124:127], v[220:223], v[124:127]\n"
    "ds_read_b64_tr_b16 a[104:105], v230 offset:32768\n"
    "ds_read_b64_tr_b16 a[106:107], v230 offset:36864\n"
    "s_nop 0\n"
    "v_mfma_f32_16x16x32_bf16 v[128:131], a[128:131], v[208:211], v[128:131]\n"
    "v_exp_f32 v183, v183\n"
    "v_mfma_f32_16x16x32_bf16 v[132:135], a[128:131], v[212:215], v[132:135]\n"
    "v_cvt_pk_bf16_f32 v199, v182, v183\n"
    "s_nop 0\n"
    "v_mfma_f32_16x16x32_bf16 v[136:139], a[128:131], v[216:219], v[136:139]\n"
    "v_exp_f32 v184, v184\n"
    "v_mfma_f32_16x16x32_bf16 v[140:143], a[128:131], v[220:223], v[140:143]\n"
    "ds_read_b64_tr_b16 a[108:109], v231 offset:32768\n"
    "ds_read_b64_tr_b16 a[110:111], v231 offset:36864\n"
    "s_nop 0\n"
    "v_mfma_f32_16x16x32_bf16 v[160:163], a[64:67], a[0:3], v[144:147]\n"
    "v_exp_f32 v185, v185\n"
    "v_mfma_f32_16x16x32_bf16 v[160:163], a[68:71], a[4:7], v[160:163]\n"
    "v_cvt_pk_bf16_f32 v202, v184, v185\n"
    "s_nop 0\n"
    "v_mfma_f32_16x16x32_bf16 v[160:163], a[72:75], a[8:11], v[160:163]\n"
    "v_exp_f32 v186, v186\n"
    "v_mfma_f32_16x16x32_bf16 v[160:163], a[76:79], a[12:15], v[160:163]\n"
    "ds_read_b128 a[80:83], v224 offset:45056\n"
    "ds_read_b128 a[84:87], v225 offset:45056\n"
    "s_nop 0\n"
    "v_mfma_f32_16x16x32_bf16 v[164:167], a[64:67], a[16:19], v[148:151]\n"
    "v_exp_f32 v187, v187\n"
    "v_mfma_f32_16x16x32_bf16 v[164:167], a[68:71], a[20:23], v[164:167]\n"
    "v_cvt_pk_bf16_f32 v203, v186, v187\n"
    "s_nop 0\n"
    "v_mfma_f32_16x16x32_bf16 v[164:167], a[72:75], a[24:27], v[164:167]\n"
    "v_exp_f32 v188, v188\n"
    "v_mfma_f32_16x16x32_bf16 v[164:167], a[76:79], a[28:31], v[164:167]\n"
    "ds_read_b128 a[88:91], v226 offset:45056\n"
    "ds_read_b128 a[92:95], v227 offset:45056\n"
    "s_nop 0\n"
    "v_mfma_f32_16x16x32_bf16 v[168:171], a[64:67], a[32:35], v[152:155]\n"
    "v_exp_f32 v189, v189\n"
    "s_memrealtime s[76:77]\n"
    "v_mfma_f32_16x16x32_bf16 v[168:171], a[68:71], a[36:39], v[168:171]\n"
    "v_cvt_pk_bf16_f32 v206, v188, v189\n"
    "s_add_u32 m0, s57, 16384\n"
    "s_nop 0\n"
    "v_mfma_f32_16x16x32_bf16 v[168:171], a[72:75], a[40:43], v[168:171]\n"
    "v_exp_f32 v190, v190\n"
    "v_mfma_f32_16x16x32_bf16 v[168:171], a[76:79], a[44:47], v[168:171]\n"
    "buffer_load_dwordx4 v236, s[40:43], s56 offen nt lds\n"
    "s_nop 0\n"
    "v_mfma_f32_16x16x32_bf16 v[172:175], a[64:67], a[48:51], v[156:159]\n"
    "v_exp_f32 v191, v191\n"
    "v_mfma_f32_16x16x32_bf16 v[172:175], a[68:71], a[52:55], v[172:175]\n"
    "v_cvt_pk_bf16_f32 v207, v190, v191\n"
    "s_nop 0\n"
    "v_mfma_f32_16x16x32_bf16 v[172:175], a[72:75], a[56:59], v[172:175]\n"
    "s_waitcnt lgkmcnt(0)\n"
    "s_sub_u32 s79, s78, s76\n"
    "v_mfma_f32_16x16x32_bf16 v[172:175], a[76:79], a[60:63], v[172:175]\n"
    "s_cmp_gt_i32 s79, 0\n"
    "s_cbranch_scc1 Lpw10_%=\n"
    "Lpw10_%=b:\n"
    "s_nop 0\n"
    "v_mfma_f32_16x16x32_bf16 v[0:3], a[96:99], v[192:195], v[0:3]\n"
    "s_max_i32 s79, s79, -12\n"
    "s_add_u32 s78, s76, s79\n"
    "v_mfma_f32_16x16x32_bf16 v[4:7], a[100:103], v[192:195], v[4:7]\n"
    "buffer_load_dwordx4 v237, s[40:43], s56 offen offset:1024 nt lds\n"
    "s_nop 0\n"
    "v_mfma_f32_16x16x32_bf16 v[8:11], a[104:107], v[192:195], v[8:11]\n"
    "v_exp_f32 v160, v160\n"
    "v_mfma_f32_16x16x32_bf16 v[12:15], a[108:111], v[192:195], v[12:15]\n"
    "ds_read_b64_tr_b16 a[112:113], v232 offset:32768\n"
    "ds_read_b64_tr_b16 a[114:115], v232 offset:36864\n"
    "s_nop 0\n"
    "v_mfma_f32_16x16x32_bf16 v[32:35], a[96:99], v[196:199], v[32:35]\n"
    "v_exp_f32 v161, v161\n"
    "v_mfma_f32_16x16x32_bf16 v[36:39], a[100:103], v[196:199], v[36:39]\n"
    "v_cvt_pk_bf16_f32 v208, v160, v161\n"
    "s_nop 0\n"
    "v_mfma_f32_16x16x32_bf16 v[40:43], a[104:107], v[196:199], v[40:43]\n"
    "v_exp_f32 v162, v162\n"
    "v_mfma_f32_16x16x32_bf16 v[44:47], a[108:111], v[196:199], v[44:47]\n"
    "ds_read_b64_tr_b16 a[116:117], v233 offset:32768\n"
    "ds_read_b64_tr_b16 a[118:119], v233 offset:36864\n"
    "s_nop 0\n"
    "v_mfma_f32_16x16x32_bf16 v[64:67], a[96:99], v[200:203], v[64:67]\n"
    "v_exp_f32 v163, v163\n"
    "v_mfma_f32_16x16x32_bf16 v[68:71], a[100:103], v[200:203], v[68:71]\n"
    "v_cvt_pk_bf16_f32 v209, v162, v163\n"
    "s_lshr_b32 s79, s80, 1\n"
    "s_add_u32 s78, s78, s79\n"
    "s_nop 0\n"
    "v_mfma_f32_16x16x32_bf16 v[72:75], a[104:107], v[200:203], v[72:75]\n"
    "v_exp_f32 v164, v164\n"
    "v_mfma_f32_16x16x32_bf16 v[76:79], a[108:111], v[200:203], v[76:79]\n"
    "ds_read_b64_tr_b16 a[120:121], v234 offset:32768\n"
    "ds_read_b64_tr_b16 a[122:123], v234 offset:36864\n"
    "s_nop 0\n"
    "v_mfma_f32_16x16x32_bf16 v[96:99], a[96:99], v[204:207], v[96:99]\n"
    "v_exp_f32 v165, v165\n"
    "v_mfma_f32_16x16x32_bf16 v[100:103], a[100:103], v[204:207], v[100:103]\n"
    "v_cvt_pk_bf16_f32 v212, v164, v165\n"
    "s_nop 0\n"
    "v_mfma_f32_16x16x32_bf16 v[104:107], a[104:107], v[204:207], v[104:107]\n"
    "v_exp_f32 v166, v166\n"
    "v_mfma_f32_16x16x32_bf16 v[108:111], a[108:111], v[204:207], v[108:111]\n"
    "ds_read_b64_tr_b16 a[124:125], v235 offset:32768\n"
    "ds_read_b64_tr_b16 a[126:127], v235 offset:36864\n"
    "s_nop 0\n"
    "v_mfma_f32_16x16x32_bf16 v[176:179], a[80:83], a[0:3], v[144:147]\n"
    "v_exp_f32 v167, v167\n"
    "v_mfma_f32_16x16x32_bf16 v[176:179], a[84:87], a[4:7], v[176:179]\n"
    "v_cvt_pk_bf16_f32 v213, v166, v167\n"
    "s_sub_u32 s80, s80, 4\n"
    "s_nop 0\n"
    "v_mfma_f32_16x16x32_bf16 v[176:179], a[88:91], a[8:11], v[176:179]\n"
    "v_exp_f32 v168, v168\n"
    "v_mfma_f32_16x16x32_bf16 v[176:179], a[92:95], a[12:15], v[176:179]\n"
    "ds_read_b128 a[64:67], v224 offset:49152\n"
    "ds_read_b128 a[68:71], v225 offset:49152\n"
    "s_nop 0\n"
    "v_mfma_f32_16x16x32_bf16 v[180:183], a[80:83], a[16:19], v[148:151]\n"
    "v_exp_f32 v169, v169\n"
    "v_mfma_f32_16x16x32_bf16 v[180:183], a[84:87], a[20:23], v[180:183]\n"
    "v_cvt_pk_bf16_f32 v216, v168, v169\n"
    "s_max_i32 s80, s80, 70\n"
    "s_nop 0\n"
    "v_mfma_f32_16x16x32_bf16 v[180:183], a[88:91], a[24:27], v[180:183]\n"
    "v_exp_f32 v170, v170\n"
    "v_mfma_f32_16x16x32_bf16 v[180:183], a[92:95], a[28:31], v[180:183]\n"
    "ds_read_b128 a[72:75], v226 offset:49152\n"
    "ds_read_b128 a[76:79], v227 offset:49152\n"
    "s_nop 0\n"
    "v_mfma_f32_16x16x32_bf16 v[184:187], a[80:83], a[32:35], v[152:155]\n"
    "v_exp_f32 v171, v171\n"
    "v_mfma_f32_16x16x32_bf16 v[184:187], a[84:87], a[36:39], v[184:187]\n"
    "v_cvt_pk_bf16_f32 v217, v170, v171\n"
    "s_memrealtime s[76:77]\n"
    "s_nop 0\n"
    "v_mfma_f32_16x16x32_bf16 v[184:187], a[88:91], a[40:43], v[184:187]\n"
    "v_exp_f32 v172, v172\n"
    "v_mfma_f32_16x16x32_bf16 v[184:187], a[92:95], a[44:47], v[184:187]\n"
    "buffer_load_dwordx4 v238, s[40:43], s56 offen offset:2048 nt lds\n"
    "s_nop 0\n"
    "v_mfma_f32_16x16x32_bf16 v[188:191], a[80:83], a[48:51], v[156:159]\n"
    "v_exp_f32 v173, v173\n"
    "v_mfma_f32_16x16x32_bf16 v[188:191], a[84:87], a[52:55], v[188:191]\n"
    "v_cvt_pk_bf16_f32 v220, v172, v173\n"
    "s_nop 0\n"
    "v_mfma_f32_16x16x32_bf16 v[188:191], a[88:91], a[56:59], v[188:191]\n"
    "v_exp_f32 v174, v174\n"
    "v_mfma_f32_16x16x32_bf16 v[188:191], a[92:95], a[60:63], v[188:191]\n"
    "s_waitcnt lgkmcnt(0)\n"
    "s_sub_u32 s79, s78, s76\n"
    "s_cmp_gt_i32 s79, 0\n"
    "s_cbranch_scc1 Lpw11_%=\n"
    "Lpw11_%=b:\n"
    "s_max_i32 s79, s79, -12\n"
    "s_add_u32 s78, s76, s79\n"
    "s_lshr_b32 s79, s80, 1\n"
    "s_add_u32 s78, s78, s79\n"
    "s_nop 0\n"
    "v_mfma_f32_16x16x32_bf16 v[16:19], a[112:115], v[192:195], v[16:19]\n"
    "v_exp_f32 v175, v175\n"
    "v_mfma_f32_16x16x32_bf16 v[20:23], a[116:119], v[192:195], v[20:23]\n"
    "buffer_load_dwordx4 v239, s[40:43], s56 offen offset:3072 nt lds\n"
    "v_cvt_pk_bf16_f32 v221, v174, v175\n"
    "s_nop 0\n"
    "v_mfma_f32_16x16x32_bf16 v[24:27], a[120:123], v[192:195], v[24:27]\n"
    "v_exp_f32 v176, v176\n"
    "v_mfma_f32_16x16x32_bf16 v[28:31], a[124:127], v[192:195], v[28:31]\n"
    "s_nop 0\n"
    "v_mfma_f32_16x16x32_bf16 v[48:51], a[112:115], v[196:199], v[48:51]\n"
    "v_exp_f32 v177, v177\n"
    "v_mfma_f32_16x16x32_bf16 v[52:55], a[116:119], v[196:199], v[52:55]\n"
    "v_cvt_pk_bf16_f32 v210, v176, v177\n"
    "s_nop 0\n"
    "v_mfma_f32_16x16x32_bf16 v[56:59], a[120:123], v[196:199], v[56:59]\n"
    "v_exp_f32 v178, v178\n"
    "v_mfma_f32_16x16x32_bf16 v[60:63], a[124:127], v[196:199], v[60:63]\n"
    "ds_read_b64_tr_b16 a[96:97], v228 offset:40960\n"
    "ds_read_b64_tr_b16 a[98:99], v228 offset:45056\n"
    "s_nop 0\n"
    "v_mfma_f32_16x16x32_bf16 v[80:83], a[112:115], v[200:203], v[80:83]\n"
    "v_exp_f32 v179, v179\n"
    "v_mfma_f32_16x16x32_bf16 v[84:87], a[116:119], v[200:203], v[84:87]\n"
    "v_cvt_pk_bf16_f32 v211, v178, v179\n"
    "s_nop 0\n"
    "v_mfma_f32_16x16x32_bf16 v[88:91], a[120:123], v[200:203], v[88:91]\n"
    "v_exp_f32 v180, v180\n"
    "v_mfma_f32_16x16x32_bf16 v[92:95], a[124:127], v[200:203], v[92:95]\n"
    "ds_read_b64_tr_b16 a[100:101], v229 offset:40960\n"
    "ds_read_b64_tr_b16 a[102:103], v229 offset:45056\n"
    "s_nop 0\n"
    "v_mfma_f32_16x16x32_bf16 v[112:115], a[112:115], v[204:207], v[112:115]\n"
    "v_exp_f32 v181, v181\n"
    "v_mfma_f32_16x16x32_bf16 v[116:119], a[116:119], v[204:207], v[116:119]\n"
    "v_cvt_pk_bf16_f32 v214, v180, v181\n"
    "s_nop 0\n"
    "v_mfma_f32_16x16x32_bf16 v[120:123], a[120:123], v[204:207], v[120:123]\n"
    "v_exp_f32 v182, v182\n"
    "v_mfma_f32_16x16x32_bf16 v[124:127], a[124:127], v[204:207], v[124:127]\n"
    "ds_read_b64_tr_b16 a[104:105], v230 offset:40960\n"
    "ds_read_b64_tr_b16 a[106:107], v230 offset:45056\n"
    "s_nop 0\n"
    "v_mfma_f32_16x16x32_bf16 v[128:131], a[128:131], v[192:195], v[128:131]\n"
    "v_exp_f32 v183, v183\n"
    "v_mfma_f32_16x16x32_bf16 v[132:135], a[128:131], v[196:199], v[132:135]\n"
    "v_cvt_pk_bf16_f32 v215, v182, v183\n"
    "s_nop 0\n"
    "v_mfma_f32_16x16x32_bf16 v[136:139], a[128:131], v[200:203], v[136:139]\n"
    "v_exp_f32 v184, v184\n"
    "v_mfma_f32_16x16x32_bf16 v[140:143], a[128:131], v[204:207], v[140:143]\n"
    "ds_read_b64_tr_b16 a[108:109], v231 offset:40960\n"
    "ds_read_b64_tr_b16 a[110:111], v231 offset:45056\n"
    "s_nop 0\n"
    "v_mfma_f32_16x16x32_bf16 v[160:163], a[64:67], a[0:3], v[144:147]\n"
    "v_exp_f32 v185, v185\n"
    "v_mfma_f32_16x16x32_bf16 v[160:163], a[68:71], a[4:7], v[160:163]\n"
    "v_cvt_pk_bf16_f32 v218, v184, v185\n"
    "s_nop 0\n"
    "v_mfma_f32_16x16x32_bf16 v[160:163], a[72:75], a[8:11], v[160:163]\n"
    "v_exp_f32 v186, v186\n"
    "v_mfma_f32_16x16x32_bf16 v[160:163], a[76:79], a[12:15], v[160:163]\n"
    "ds_read_b128 a[80:83], v224 offset:53248\n"
    "ds_read_b128 a[84:87], v225 offset:53248\n"
    "s_nop 0\n"
    "v_mfma_f32_16x16x32_bf16 v[164:167], a[64:67], a[16:19], v[148:151]\n"
    "v_exp_f32 v187, v187\n"
    "v_mfma_f32_16x16x32_bf16 v[164:167], a[68:71], a[20:23], v[164:167]\n"
    "v_cvt_pk_bf16_f32 v219, v186, v187\n"
    "s_nop 0\n"
    "v_mfma_f32_16x16x32_bf16 v[164:167], a[72:75], a[24:27], v[164:167]\n"
    "v_exp_f32 v188, v188\n"
    "v_mfma_f32_16x16x32_bf16 v[164:167], a[76:79], a[28:31], v[164:167]\n"
    "ds_read_b128 a[88:91], v226 offset:53248\n"
    "ds_read_b128 a[92:95], v227 offset:53248\n"
    "s_nop 0\n"
    "v_mfma_f32_16x16x32_bf16 v[168:171], a[64:67], a[32:35], v[152:155]\n"
    "v_exp_f32 v189, v189\n"
    "s_memrealtime s[76:77]\n"
    "v_mfma_f32_16x16x32_bf16 v[168:171], a[68:71], a[36:39], v[168:171]\n"
    "v_cvt_pk_bf16_f32 v222, v188, v189\n"
    "s_add_u32 m0, s57, 81920\n"
    "s_nop 0\n"
    "v_mfma_f32_16x16x32_bf16 v[168:171], a[72:75], a[40:43], v[168:171]\n"
    "v_exp_f32 v190, v190\n"
    "v_mfma_f32_16x16x32_bf16 v[168:171], a[76:79], a[44:47], v[168:171]\n"
    "buffer_load_dwordx4 v240, s[44:47], s56 offen nt lds\n"
    "s_nop 0\n"
    "v_mfma_f32_16x16x32_bf16 v[172:175], a[64:67], a[48:51], v[156:159]\n"
    "v_exp_f32 v191, v191\n"
    "v_mfma_f32_16x16x32_bf16 v[172:175], a[68:71], a[52:55], v[172:175]\n"
    "v_cvt_pk_bf16_f32 v223, v190, v191\n"
    "s_nop 0\n"
    "v_mfma_f32_16x16x32_bf16 v[172:175], a[72:75], a[56:59], v[172:175]\n"
    "s_waitcnt lgkmcnt(0)\n"
    "s_sub_u32 s79, s78, s76\n"
    "v_mfma_f32_16x16x32_bf16 v[172:175], a[76:79], a[60:63], v[172:175]\n"
    "s_cmp_gt_i32 s79, 0\n"
    "s_cbranch_scc1 Lpw12_%=\n"
    "Lpw12_%=b:\n"
    "s_nop 0\n"
    "v_mfma_f32_16x16x32_bf16 v[0:3], a[96:99], v[208:211], v[0:3]\n"
    "s_max_i32 s79, s79, -12\n"
    "s_add_u32 s78, s76, s79\n"
    "v_mfma_f32_16x16x32_bf16 v[4:7], a[100:103], v[208:211], v[4:7]\n"
    "buffer_load_dwordx4 v241, s[44:47], s56 offen offset:1024 nt lds\n"
    "s_nop 0\n"
    "v_mfma_f32_16x16x32_bf16 v[8:11], a[104:107], v[208:211], v[8:11]\n"
    "v_exp_f32 v160, v160\n"
    "v_mfma_f32_16x16x32_bf16 v[12:15], a[108:111], v[208:211], v[12:15]\n"
    "ds_read_b64_tr_b16 a[112:113], v232 offset:40960\n"
    "ds_read_b64_tr_b16 a[114:115], v232 offset:45056\n"
    "s_nop 0\n"
    "v_mfma_f32_16x16x32_bf16 v[32:35], a[96:99], v[212:215], v[32:35]\n"
    "v_exp_f32 v161, v161\n"
    "v_mfma_f32_16x16x32_bf16 v[36:39], a[100:103], v[212:215], v[36:39]\n"
    "v_cvt_pk_bf16_f32 v192, v160, v161\n"
    "s_nop 0\n"
    "v_mfma_f32_16x16x32_bf16 v[40:43], a[104:107], v[212:215], v[40:43]\n"
    "v_exp_f32 v162, v162\n"
    "v_mfma_f32_16x16x32_bf16 v[44:47], a[108:111], v[212:215], v[44:47]\n"
    "ds_read_b64_tr_b16 a[116:117], v233 offset:40960\n"
    "ds_read_b64_tr_b16 a[118:119], v233 offset:45056\n"
    "s_nop 0\n"
    "v_mfma_f32_16x16x32_bf16 v[64:67], a[96:99], v[216:219], v[64:67]\n"
    "v_exp_f32 v163, v163\n"
    "v_mfma_f32_16x16x32_bf16 v[68:71], a[100:103], v[216:219], v[68:71]\n"
    "v_cvt_pk_bf16_f32 v193, v162, v163\n"
    "s_lshr_b32 s79, s80, 1\n"
    "s_add_u32 s78, s78, s79\n"
    "s_nop 0\n"
    "v_mfma_f32_16x16x32_bf16 v[72:75], a[104:107], v[216:219], v[72:75]\n"
    "v_exp_f32 v164, v164\n"
    "v_mfma_f32_16x16x32_bf16 v[76:79], a[108:111], v[216:219], v[76:79]\n"
    "ds_read_b64_tr_b16 a[120:121], v234 offset:40960\n"
    "ds_read_b64_tr_b16 a[122:123], v234 offset:45056\n"
    "s_nop 0\n"
    "v_mfma_f32_16x16x32_bf16 v[96:99], a[96:99], v[220:223], v[96:99]\n"
    "v_exp_f32 v165, v165\n"
    "v_mfma_f32_16x16x32_bf16 v[100:103], a[100:103], v[220:223], v[100:103]\n"
    "v_cvt_pk_bf16_f32 v196, v164, v165\n"
    "s_nop 0\n"
    "v_mfma_f32_16x16x32_bf16 v[104:107], a[104:107], v[220:223], v[104:107]\n"
    "v_exp_f32 v166, v166\n"
    "v_mfma_f32_16x16x32_bf16 v[108:111], a[108:111], v[220:223], v[108:111]\n"
    "ds_read_b64_tr_b16 a[124:125], v235 offset:40960\n"
    "ds_read_b64_tr_b16 a[126:127], v235 offset:45056\n"
    "s_nop 0\n"
    "v_mfma_f32_16x16x32_bf16 v[176:179], a[80:83], a[0:3], v[144:147]\n"
    "v_exp_f32 v167, v167\n"
    "v_mfma_f32_16x16x32_bf16 v[176:179], a[84:87], a[4:7], v[176:179]\n"
    "v_cvt_pk_bf16_f32 v197, v166, v167\n"
    "s_sub_u32 s80, s80, 4\n"
    "s_nop 0\n"
    "v_mfma_f32_16x16x32_bf16 v[176:179], a[88:91], a[8:11], v[176:179]\n"
    "v_exp_f32 v168, v168\n"
    "v_mfma_f32_16x16x32_bf16 v[176:179], a[92:95], a[12:15], v[176:179]\n"
    "ds_read_b128 a[64:67], v224 offset:57344\n"
    "ds_read_b128 a[68:71], v225 offset:57344\n"
    "s_nop 0\n"
    "v_mfma_f32_16x16x32_bf16 v[180:183], a[80:83], a[16:19], v[148:151]\n"
    "v_exp_f32 v169, v169\n"
    "v_mfma_f32_16x16x32_bf16 v[180:183], a[84:87], a[20:23], v[180:183]\n"
    "v_cvt_pk_bf16_f32 v200, v168, v169\n"
    "s_max_i32 s80, s80, 70\n"
    "s_nop 0\n"
    "v_mfma_f32_16x16x32_bf16 v[180:183], a[88:91], a[24:27], v[180:183]\n"
    "v_exp_f32 v170, v170\n"
    "v_mfma_f32_16x16x32_bf16 v[180:183], a[92:95], a[28:31], v[180:183]\n"
    "ds_read_b128 a[72:75], v226 offset:57344\n"
    "ds_read_b128 a[76:79], v227 offset:57344\n"
    "s_nop 0\n"
    "v_mfma_f32_16x16x32_bf16 v[184:187], a[80:83], a[32:35], v[152:155]\n"
    "v_exp_f32 v171, v171\n"
    "v_mfma_f32_16x16x32_bf16 v[184:187], a[84:87], a[36:39], v[184:187]\n"
    "v_cvt_pk_bf16_f32 v201, v170, v171\n"
    "s_memrealtime s[76:77]\n"
    "s_nop 0\n"
    "v_mfma_f32_16x16x32_bf16 v[184:187], a[88:91], a[40:43], v[184:187]\n"
    "v_exp_f32 v172, v172\n"
    "v_mfma_f32_16x16x32_bf16 v[184:187], a[92:95], a[44:47], v[184:187]\n"
    "buffer_load_dwordx4 v240, s[44:47], s56 offen offset:2048 nt lds\n"
    "s_nop 0\n"
    "v_mfma_f32_16x16x32_bf16 v[188:191], a[80:83], a[48:51], v[156:159]\n"
    "v_exp_f32 v173, v173\n"
    "v_mfma_f32_16x16x32_bf16 v[188:191], a[84:87], a[52:55], v[188:191]\n"
    "v_cvt_pk_bf16_f32 v204, v172, v173\n"
    "s_nop 0\n"
    "v_mfma_f32_16x16x32_bf16 v[188:191], a[88:91], a[56:59], v[188:191]\n"
    "v_exp_f32 v174, v174\n"
    "v_mfma_f32_16x16x32_bf16 v[188:191], a[92:95], a[60:63], v[188:191]\n"
    "s_waitcnt lgkmcnt(0)\n"
    "s_sub_u32 s79, s78, s76\n"
    "s_cmp_gt_i32 s79, 0\n"
    "s_cbranch_scc1 Lpw13_%=\n"
    "Lpw13_%=b:\n"
    "s_max_i32 s79, s79, -12\n"
    "s_add_u32 s78, s76, s79\n"
    "s_lshr_b32 s79, s80, 1\n"
    "s_add_u32 s78, s78, s79\n"
    "s_nop 0\n"
    "v_mfma_f32_16x16x32_bf16 v[16:19], a[112:115], v[208:211], v[16:19]\n"
    "v_exp_f32 v175, v175\n"
    "v_mfma_f32_16x16x32_bf16 v[20:23], a[116:119], v[208:211], v[20:23]\n"
    "buffer_load_dwordx4 v241, s[44:47], s56 offen offset:3072 nt lds\n"
    "v_cvt_pk_bf16_f32 v205, v174, v175\n"
    "s_add_u32 s56, s56, 16384\n"
    "s_nop 0\n"
    "v_mfma_f32_16x16x32_bf16 v[24:27], a[120:123], v[208:211], v[24:27]\n"
    "v_exp_f32 v176, v176\n"
    "v_mfma_f32_16x16x32_bf16 v[28:31], a[124:127], v[208:211], v[28:31]\n"
    "s_waitcnt vmcnt(12)\n"
    "s_barrier\n"
    "s_nop 0\n"
    "v_mfma_f32_16x16x32_bf16 v[48:51], a[112:115], v[212:215], v[48:51]\n"
    "v_exp_f32 v177, v177\n"
    "v_mfma_f32_16x16x32_bf16 v[52:55], a[116:119], v[212:215], v[52:55]\n"
    "v_cvt_pk_bf16_f32 v194, v176, v177\n"
    "s_nop 0\n"
    "v_mfma_f32_16x16x32_bf16 v[56:59], a[120:123], v[212:215], v[56:59]\n"
    "v_exp_f32 v178, v178\n"
    "v_mfma_f32_16x16x32_bf16 v[60:63], a[124:127], v[212:215], v[60:63]\n"
    "ds_read_b64_tr_b16 a[96:97], v228 offset:49152\n"
    "ds_read_b64_tr_b16 a[98:99], v228 offset:53248\n"
    "s_nop 0\n"
    "v_mfma_f32_16x16x32_bf16 v[80:83], a[112:115], v[216:219], v[80:83]\n"
    "v_exp_f32 v179, v179\n"
    "v_mfma_f32_16x16x32_bf16 v[84:87], a[116:119], v[216:219], v[84:87]\n"
    "v_cvt_pk_bf16_f32 v195, v178, v179\n"
    "s_nop 0\n"
    "v_mfma_f32_16x16x32_bf16 v[88:91], a[120:123], v[216:219], v[88:91]\n"
    "v_exp_f32 v180, v180\n"
    "v_mfma_f32_16x16x32_bf16 v[92:95], a[124:127], v[216:219], v[92:95]\n"
    "ds_read_b64_tr_b16 a[100:101], v229 offset:49152\n"
    "ds_read_b64_tr_b16 a[102:103], v229 offset:53248\n"
    "s_nop 0\n"
    "v_mfma_f32_16x16x32_bf16 v[112:115], a[112:115], v[220:223], v[112:115]\n"
    "v_exp_f32 v181, v181\n"
    "v_mfma_f32_16x16x32_bf16 v[116:119], a[116:119], v[220:223], v[116:119]\n"
    "v_cvt_pk_bf16_f32 v198, v180, v181\n"
    "s_nop 0\n"
    "v_mfma_f32_16x16x32_bf16 v[120:123], a[120:123], v[220:223], v[120:123]\n"
    "v_exp_f32 v182, v182\n"
    "v_mfma_f32_16x16x32_bf16 v[124:127], a[124:127], v[220:223], v[124:127]\n"
    "ds_read_b64_tr_b16 a[104:105], v230 offset:49152\n"
    "ds_read_b64_tr_b16 a[106:107], v230 offset:53248\n"
    "s_nop 0\n"
    "v_mfma_f32_16x16x32_bf16 v[128:131], a[128:131], v[208:211], v[128:131]\n"
    "v_exp_f32 v183, v183\n"
    "v_mfma_f32_16x16x32_bf16 v[132:135], a[128:131], v[212:215], v[132:135]\n"
    "v_cvt_pk_bf16_f32 v199, v182, v183\n"
    "s_nop 0\n"
    "v_mfma_f32_16x16x32_bf16 v[136:139], a[128:131], v[216:219], v[136:139]\n"
    "v_exp_f32 v184, v184\n"
    "v_mfma_f32_16x16x32_bf16 v[140:143], a[128:131], v[220:223], v[140:143]\n"
    "ds_read_b64_tr_b16 a[108:109], v231 offset:49152\n"
    "ds_read_b64_tr_b16 a[110:111], v231 offset:53248\n"
    "s_nop 0\n"
    "v_mfma_f32_16x16x32_bf16 v[160:163], a[64:67], a[0:3], v[144:147]\n"
    "v_exp_f32 v185, v185\n"
    "v_mfma_f32_16x16x32_bf16 v[160:163], a[68:71], a[4:7], v[160:163]\n"
    "v_cvt_pk_bf16_f32 v202, v184, v185\n"
    "s_nop 0\n"
    "v_mfma_f32_16x16x32_bf16 v[160:163], a[72:75], a[8:11], v[160:163]\n"
    "v_exp_f32 v186, v186\n"
    "v_mfma_f32_16x16x32_bf16 v[160:163], a[76:79], a[12:15], v[160:163]\n"
    "ds_read_b128 a[80:83], v224 offset:61440\n"
    "ds_read_b128 a[84:87], v225 offset:61440\n"
    "s_nop 0\n"
    "v_mfma_f32_16x16x32_bf16 v[164:167], a[64:67], a[16:19], v[148:151]\n"
    "v_exp_f32 v187, v187\n"
    "v_mfma_f32_16x16x32_bf16 v[164:167], a[68:71], a[20:23], v[164:167]\n"
    "v_cvt_pk_bf16_f32 v203, v186, v187\n"
    "s_nop 0\n"
    "v_mfma_f32_16x16x32_bf16 v[164:167], a[72:75], a[24:27], v[164:167]\n"
    "v_exp_f32 v188, v188\n"
    "v_mfma_f32_16x16x32_bf16 v[164:167], a[76:79], a[28:31], v[164:167]\n"
    "ds_read_b128 a[88:91], v226 offset:61440\n"
    "ds_read_b128 a[92:95], v227 offset:61440\n"
    "s_nop 0\n"
    "v_mfma_f32_16x16x32_bf16 v[168:171], a[64:67], a[32:35], v[152:155]\n"
    "v_exp_f32 v189, v189\n"
    "s_memrealtime s[76:77]\n"
    "v_mfma_f32_16x16x32_bf16 v[168:171], a[68:71], a[36:39], v[168:171]\n"
    "v_cvt_pk_bf16_f32 v206, v188, v189\n"
    "s_add_u32 m0, s57, 32768\n"
    "s_nop 0\n"
    "v_mfma_f32_16x16x32_bf16 v[168:171], a[72:75], a[40:43], v[168:171]\n"
    "v_exp_f32 v190, v190\n"
    "v_mfma_f32_16x16x32_bf16 v[168:171], a[76:79], a[44:47], v[168:171]\n"
    "buffer_load_dwordx4 v236, s[40:43], s56 offen nt lds\n"
    "s_nop 0\n"
    "v_mfma_f32_16x16x32_bf16 v[172:175], a[64:67], a[48:51], v[156:159]\n"
    "v_exp_f32 v191, v191\n"
    "v_mfma_f32_16x16x32_bf16 v[172:175], a[68:71], a[52:55], v[172:175]\n"
    "v_cvt_pk_bf16_f32 v207, v190, v191\n"
    "s_nop 0\n"
    "v_mfma_f32_16x16x32_bf16 v[172:175], a[72:75], a[56:59], v[172:175]\n"
    "s_waitcnt lgkmcnt(0)\n"
    "s_sub_u32 s79, s78, s76\n"
    "v_mfma_f32_16x16x32_bf16 v[172:175], a[76:79], a[60:63], v[172:175]\n"
    "s_cmp_gt_i32 s79, 0\n"
    "s_cbranch_scc1 Lpw14_%=\n"
    "Lpw14_%=b:\n"
    "s_nop 0\n"
    "v_mfma_f32_16x16x32_bf16 v[0:3], a[96:99], v[192:195], v[0:3]\n"
    "s_max_i32 s79, s79, -12\n"
    "s_add_u32 s78, s76, s79\n"
    "v_mfma_f32_16x16x32_bf16 v[4:7], a[100:103], v[192:195], v[4:7]\n"
    "buffer_load_dwordx4 v237, s[40:43], s56 offen offset:1024 nt lds\n"
    "s_nop 0\n"
    "v_mfma_f32_16x16x32_bf16 v[8:11], a[104:107], v[192:195], v[8:11]\n"
    "v_exp_f32 v160, v160\n"
    "v_mfma_f32_16x16x32_bf16 v[12:15], a[108:111], v[192:195], v[12:15]\n"
    "ds_read_b64_tr_b16 a[112:113], v232 offset:49152\n"
    "ds_read_b64_tr_b16 a[114:115], v232 offset:53248\n"
    "s_nop 0\n"
    "v_mfma_f32_16x16x32_bf16 v[32:35], a[96:99], v[196:199], v[32:35]\n"
    "v_exp_f32 v161, v161\n"
    "v_mfma_f32_16x16x32_bf16 v[36:39], a[100:103], v[196:199], v[36:39]\n"
    "v_cvt_pk_bf16_f32 v208, v160, v161\n"
    "s_nop 0\n"
    "v_mfma_f32_16x16x32_bf16 v[40:43], a[104:107], v[196:199], v[40:43]\n"
    "v_exp_f32 v162, v162\n"
    "v_mfma_f32_16x16x32_bf16 v[44:47], a[108:111], v[196:199], v[44:47]\n"
    "ds_read_b64_tr_b16 a[116:117], v233 offset:49152\n"
    "ds_read_b64_tr_b16 a[118:119], v233 offset:53248\n"
    "s_nop 0\n"
    "v_mfma_f32_16x16x32_bf16 v[64:67], a[96:99], v[200:203], v[64:67]\n"
    "v_exp_f32 v163, v163\n"
    "v_mfma_f32_16x16x32_bf16 v[68:71], a[100:103], v[200:203], v[68:71]\n"
    "v_cvt_pk_bf16_f32 v209, v162, v163\n"
    "s_lshr_b32 s79, s80, 1\n"
    "s_add_u32 s78, s78, s79\n"
    "s_nop 0\n"
    "v_mfma_f32_16x16x32_bf16 v[72:75], a[104:107], v[200:203], v[72:75]\n"
    "v_exp_f32 v164, v164\n"
    "v_mfma_f32_16x16x32_bf16 v[76:79], a[108:111], v[200:203], v[76:79]\n"
    "ds_read_b64_tr_b16 a[120:121], v234 offset:49152\n"
    "ds_read_b64_tr_b16 a[122:123], v234 offset:53248\n"
    "s_nop 0\n"
    "v_mfma_f32_16x16x32_bf16 v[96:99], a[96:99], v[204:207], v[96:99]\n"
    "v_exp_f32 v165, v165\n"
    "v_mfma_f32_16x16x32_bf16 v[100:103], a[100:103], v[204:207], v[100:103]\n"
    "v_cvt_pk_bf16_f32 v212, v164, v165\n"
    "s_nop 0\n"
    "v_mfma_f32_16x16x32_bf16 v[104:107], a[104:107], v[204:207], v[104:107]\n"
    "v_exp_f32 v166, v166\n"
    "v_mfma_f32_16x16x32_bf16 v[108:111], a[108:111], v[204:207], v[108:111]\n"
    "ds_read_b64_tr_b16 a[124:125], v235 offset:49152\n"
    "ds_read_b64_tr_b16 a[126:127], v235 offset:53248\n"
    "s_nop 0\n"
    "v_mfma_f32_16x16x32_bf16 v[176:179], a[80:83], a[0:3], v[144:147]\n"
    "v_exp_f32 v167, v167\n"
    "v_mfma_f32_16x16x32_bf16 v[176:179], a[84:87], a[4:7], v[176:179]\n"
    "v_cvt_pk_bf16_f32 v213, v166, v167\n"
    "s_sub_u32 s80, s80, 4\n"
    "s_nop 0\n"
    "v_mfma_f32_16x16x32_bf16 v[176:179], a[88:91], a[8:11], v[176:179]\n"
    "v_exp_f32 v168, v168\n"
    "v_mfma_f32_16x16x32_bf16 v[176:179], a[92:95], a[12:15], v[176:179]\n"
    "ds_read_b128 a[64:67], v224\n"
    "ds_read_b128 a[68:71], v225\n"
    "s_nop 0\n"
    "v_mfma_f32_16x16x32_bf16 v[180:183], a[80:83], a[16:19], v[148:151]\n"
    "v_exp_f32 v169, v169\n"
    "v_mfma_f32_16x16x32_bf16 v[180:183], a[84:87], a[20:23], v[180:183]\n"
    "v_cvt_pk_bf16_f32 v216, v168, v169\n"
    "s_max_i32 s80, s80, 70\n"
    "s_nop 0\n"
    "v_mfma_f32_16x16x32_bf16 v[180:183], a[88:91], a[24:27], v[180:183]\n"
    "v_exp_f32 v170, v170\n"
    "v_mfma_f32_16x16x32_bf16 v[180:183], a[92:95], a[28:31], v[180:183]\n"
    "ds_read_b128 a[72:75], v226\n"
    "ds_read_b128 a[76:79], v227\n"
    "s_nop 0\n"
    "v_mfma_f32_16x16x32_bf16 v[184:187], a[80:83], a[32:35], v[152:155]\n"
    "v_exp_f32 v171, v171\n"
    "v_mfma_f32_16x16x32_bf16 v[184:187], a[84:87], a[36:39], v[184:187]\n"
    "v_cvt_pk_bf16_f32 v217, v170, v171\n"
    "s_memrealtime s[76:77]\n"
    "s_nop 0\n"
    "v_mfma_f32_16x16x32_bf16 v[184:187], a[88:91], a[40:43], v[184:187]\n"
    "v_exp_f32 v172, v172\n"
    "v_mfma_f32_16x16x32_bf16 v[184:187], a[92:95], a[44:47], v[184:187]\n"
    "buffer_load_dwordx4 v238, s[40:43], s56 offen offset:2048 nt lds\n"
    "s_nop 0\n"
    "v_mfma_f32_16x16x32_bf16 v[188:191], a[80:83], a[48:51], v[156:159]\n"
    "v_exp_f32 v173, v173\n"
    "v_mfma_f32_16x16x32_bf16 v[188:191], a[84:87], a[52:55], v[188:191]\n"
    "v_cvt_pk_bf16_f32 v220, v172, v173\n"
    "s_nop 0\n"
    "v_mfma_f32_16x16x32_bf16 v[188:191], a[88:91], a[56:59], v[188:191]\n"
    "v_exp_f32 v174, v174\n"
    "v_mfma_f32_16x16x32_bf16 v[188:191], a[92:95], a[60:63], v[188:191]\n"
    "s_waitcnt lgkmcnt(0)\n"
    "s_sub_u32 s79, s78, s76\n"
    "s_cmp_gt_i32 s79, 0\n"
    "s_cbranch_scc1 Lpw15_%=\n"
    "Lpw15_%=b:\n"
    "s_max_i32 s79, s79, -12\n"
    "s_add_u32 s78, s76, s79\n"
    "s_lshr_b32 s79, s80, 1\n"
    "s_add_u32 s78, s78, s79\n"
    "s_nop 0\n"
    "v_mfma_f32_16x16x32_bf16 v[16:19], a[112:115], v[192:195], v[16:19]\n"
    "v_exp_f32 v175, v175\n"
    "v_mfma_f32_16x16x32_bf16 v[20:23], a[116:119], v[192:195], v[20:23]\n"
    "buffer_load_dwordx4 v239, s[40:43], s56 offen offset:3072 nt lds\n"
    "v_cvt_pk_bf16_f32 v221, v174, v175\n"
    "s_nop 0\n"
    "v_mfma_f32_16x16x32_bf16 v[24:27], a[120:123], v[192:195], v[24:27]\n"
    "v_exp_f32 v176, v176\n"
    "v_mfma_f32_16x16x32_bf16 v[28:31], a[124:127], v[192:195], v[28:31]\n"
    "s_nop 0\n"
    "v_mfma_f32_16x16x32_bf16 v[48:51], a[112:115], v[196:199], v[48:51]\n"
    "v_exp_f32 v177, v177\n"
    "v_mfma_f32_16x16x32_bf16 v[52:55], a[116:119], v[196:199], v[52:55]\n"
    "v_cvt_pk_bf16_f32 v210, v176, v177\n"
    "s_nop 0\n"
    "v_mfma_f32_16x16x32_bf16 v[56:59], a[120:123], v[196:199], v[56:59]\n"
    "v_exp_f32 v178, v178\n"
    "v_mfma_f32_16x16x32_bf16 v[60:63], a[124:127], v[196:199], v[60:63]\n"
    "ds_read_b64_tr_b16 a[96:97], v228 offset:57344\n"
    "ds_read_b64_tr_b16 a[98:99], v228 offset:61440\n"
    "s_nop 0\n"
    "v_mfma_f32_16x16x32_bf16 v[80:83], a[112:115], v[200:203], v[80:83]\n"
    "v_exp_f32 v179, v179\n"
    "v_mfma_f32_16x16x32_bf16 v[84:87], a[116:119], v[200:203], v[84:87]\n"
    "v_cvt_pk_bf16_f32 v211, v178, v179\n"
    "s_nop 0\n"
    "v_mfma_f32_16x16x32_bf16 v[88:91], a[120:123], v[200:203], v[88:91]\n"
    "v_exp_f32 v180, v180\n"
    "v_mfma_f32_16x16x32_bf16 v[92:95], a[124:127], v[200:203], v[92:95]\n"
    "ds_read_b64_tr_b16 a[100:101], v229 offset:57344\n"
    "ds_read_b64_tr_b16 a[102:103], v229 offset:61440\n"
    "s_nop 0\n"
    "v_mfma_f32_16x16x32_bf16 v[112:115], a[112:115], v[204:207], v[112:115]\n"
    "v_exp_f32 v181, v181\n"
    "v_mfma_f32_16x16x32_bf16 v[116:119], a[116:119], v[204:207], v[116:119]\n"
    "v_cvt_pk_bf16_f32 v214, v180, v181\n"
    "s_nop 0\n"
    "v_mfma_f32_16x16x32_bf16 v[120:123], a[120:123], v[204:207], v[120:123]\n"
    "v_exp_f32 v182, v182\n"
    "v_mfma_f32_16x16x32_bf16 v[124:127], a[124:127], v[204:207], v[124:127]\n"
    "ds_read_b64_tr_b16 a[104:105], v230 offset:57344\n"
    "ds_read_b64_tr_b16 a[106:107], v230 offset:61440\n"
    "s_nop 0\n"
    "v_mfma_f32_16x16x32_bf16 v[128:131], a[128:131], v[192:195], v[128:131]\n"
    "v_exp_f32 v183, v183\n"
    "v_mfma_f32_16x16x32_bf16 v[132:135], a[128:131], v[196:199], v[132:135]\n"
    "v_cvt_pk_bf16_f32 v215, v182, v183\n"
    "s_nop 0\n"
    "v_mfma_f32_16x16x32_bf16 v[136:139], a[128:131], v[200:203], v[136:139]\n"
    "v_exp_f32 v184, v184\n"
    "v_mfma_f32_16x16x32_bf16 v[140:143], a[128:131], v[204:207], v[140:143]\n"
    "ds_read_b64_tr_b16 a[108:109], v231 offset:57344\n"
    "ds_read_b64_tr_b16 a[110:111], v231 offset:61440\n"
    "s_nop 0\n"
    "s_cmp_eq_u32 s58, 1\n"
    "s_cbranch_scc1 Ltail_%=\n"
    "v_mfma_f32_16x16x32_bf16 v[160:163], a[64:67], a[0:3], v[144:147]\n"
    "v_exp_f32 v185, v185\n"
    "v_mfma_f32_16x16x32_bf16 v[160:163], a[68:71], a[4:7], v[160:163]\n"
    "v_cvt_pk_bf16_f32 v218, v184, v185\n"
    "s_nop 0\n"
    "v_mfma_f32_16x16x32_bf16 v[160:163], a[72:75], a[8:11], v[160:163]\n"
    "v_exp_f32 v186, v186\n"
    "v_mfma_f32_16x16x32_bf16 v[160:163], a[76:79], a[12:15], v[160:163]\n"
    "ds_read_b128 a[80:83], v224 offset:4096\n"
    "ds_read_b128 a[84:87], v225 offset:4096\n"
    "s_nop 0\n"
    "v_mfma_f32_16x16x32_bf16 v[164:167], a[64:67], a[16:19], v[148:151]\n"
    "v_exp_f32 v187, v187\n"
    "v_mfma_f32_16x16x32_bf16 v[164:167], a[68:71], a[20:23], v[164:167]\n"
    "v_cvt_pk_bf16_f32 v219, v186, v187\n"
    "s_nop 0\n"
    "v_mfma_f32_16x16x32_bf16 v[164:167], a[72:75], a[24:27], v[164:167]\n"
    "v_exp_f32 v188, v188\n"
    "v_mfma_f32_16x16x32_bf16 v[164:167], a[76:79], a[28:31], v[164:167]\n"
    "ds_read_b128 a[88:91], v226 offset:4096\n"
    "ds_read_b128 a[92:95], v227 offset:4096\n"
    "s_nop 0\n"
    "v_mfma_f32_16x16x32_bf16 v[168:171], a[64:67], a[32:35], v[152:155]\n"
    "v_exp_f32 v189, v189\n"
    "s_memrealtime s[76:77]\n"
    "v_mfma_f32_16x16x32_bf16 v[168:171], a[68:71], a[36:39], v[168:171]\n"
    "v_cvt_pk_bf16_f32 v222, v188, v189\n"
    "s_add_u32 m0, s57, 98304\n"
    "s_nop 0\n"
    "v_mfma_f32_16x16x32_bf16 v[168:171], a[72:75], a[40:43], v[168:171]\n"
    "v_exp_f32 v190, v190\n"
    "v_mfma_f32_16x16x32_bf16 v[168:171], a[76:79], a[44:47], v[168:171]\n"
    "buffer_load_dwordx4 v240, s[44:47], s56 offen nt lds\n"
    "s_nop 0\n"
    "v_mfma_f32_16x16x32_bf16 v[172:175], a[64:67], a[48:51], v[156:159]\n"
    "v_exp_f32 v191, v191\n"
    "v_mfma_f32_16x16x32_bf16 v[172:175], a[68:71], a[52:55], v[172:175]\n"
    "v_cvt_pk_bf16_f32 v223, v190, v191\n"
    "s_nop 0\n"
    "v_mfma_f32_16x16x32_bf16 v[172:175], a[72:75], a[56:59], v[172:175]\n"
    "s_waitcnt lgkmcnt(0)\n"
    "s_sub_u32 s79, s78, s76\n"
    "v_mfma_f32_16x16x32_bf16 v[172:175], a[76:79], a[60:63], v[172:175]\n"
    "s_cmp_gt_i32 s79, 0\n"
    "s_cbranch_scc1 Lpw16_%=\n"
    "Lpw16_%=b:\n"
    "s_nop 0\n"
    "v_mfma_f32_16x16x32_bf16 v[0:3], a[96:99], v[208:211], v[0:3]\n"
    "s_max_i32 s79, s79, -12\n"
    "s_add_u32 s78, s76, s79\n"
    "v_mfma_f32_16x16x32_bf16 v[4:7], a[100:103], v[208:211], v[4:7]\n"
    "buffer_load_dwordx4 v241, s[44:47], s56 offen offset:1024 nt lds\n"
    "s_nop 0\n"
    "v_mfma_f32_16x16x32_bf16 v[8:11], a[104:107], v[208:211], v[8:11]\n"
    "v_exp_f32 v160, v160\n"
    "v_mfma_f32_16x16x32_bf16 v[12:15], a[108:111], v[208:211], v[12:15]\n"
    "ds_read_b64_tr_b16 a[112:113], v232 offset:57344\n"
    "ds_read_b64_tr_b16 a[114:115], v232 offset:61440\n"
    "s_nop 0\n"
    "v_mfma_f32_16x16x32_bf16 v[32:35], a[96:99], v[212:215], v[32:35]\n"
    "v_exp_f32 v161, v161\n"
    "v_mfma_f32_16x16x32_bf16 v[36:39], a[100:103], v[212:215], v[36:39]\n"
    "v_cvt_pk_bf16_f32 v192, v160, v161\n"
    "s_nop 0\n"
    "v_mfma_f32_16x16x32_bf16 v[40:43], a[104:107], v[212:215], v[40:43]\n"
    "v_exp_f32 v162, v162\n"
    "v_mfma_f32_16x16x32_bf16 v[44:47], a[108:111], v[212:215], v[44:47]\n"
    "ds_read_b64_tr_b16 a[116:117], v233 offset:57344\n"
    "ds_read_b64_tr_b16 a[118:119], v233 offset:61440\n"
    "s_nop 0\n"
    "v_mfma_f32_16x16x32_bf16 v[64:67], a[96:99], v[216:219], v[64:67]\n"
    "v_exp_f32 v163, v163\n"
    "v_mfma_f32_16x16x32_bf16 v[68:71], a[100:103], v[216:219], v[68:71]\n"
    "v_cvt_pk_bf16_f32 v193, v162, v163\n"
    "s_lshr_b32 s79, s80, 1\n"
    "s_add_u32 s78, s78, s79\n"
    "s_nop 0\n"
    "v_mfma_f32_16x16x32_bf16 v[72:75], a[104:107], v[216:219], v[72:75]\n"
    "v_exp_f32 v164, v164\n"
    "v_mfma_f32_16x16x32_bf16 v[76:79], a[108:111], v[216:219], v[76:79]\n"
    "ds_read_b64_tr_b16 a[120:121], v234 offset:57344\n"
    "ds_read_b64_tr_b16 a[122:123], v234 offset:61440\n"
    "s_nop 0\n"
    "v_mfma_f32_16x16x32_bf16 v[96:99], a[96:99], v[220:223], v[96:99]\n"
    "v_exp_f32 v165, v165\n"
    "v_mfma_f32_16x16x32_bf16 v[100:103], a[100:103], v[220:223], v[100:103]\n"
    "v_cvt_pk_bf16_f32 v196, v164, v165\n"
    "s_nop 0\n"
    "v_mfma_f32_16x16x32_bf16 v[104:107], a[104:107], v[220:223], v[104:107]\n"
    "v_exp_f32 v166, v166\n"
    "v_mfma_f32_16x16x32_bf16 v[108:111], a[108:111], v[220:223], v[108:111]\n"
    "ds_read_b64_tr_b16 a[124:125], v235 offset:57344\n"
    "ds_read_b64_tr_b16 a[126:127], v235 offset:61440\n"
    "s_nop 0\n"
    "v_mfma_f32_16x16x32_bf16 v[176:179], a[80:83], a[0:3], v[144:147]\n"
    "v_exp_f32 v167, v167\n"
    "v_mfma_f32_16x16x32_bf16 v[176:179], a[84:87], a[4:7], v[176:179]\n"
    "v_cvt_pk_bf16_f32 v197, v166, v167\n"
    "s_sub_u32 s80, s80, 4\n"
    "s_nop 0\n"
    "v_mfma_f32_16x16x32_bf16 v[176:179], a[88:91], a[8:11], v[176:179]\n"
    "v_exp_f32 v168, v168\n"
    "v_mfma_f32_16x16x32_bf16 v[176:179], a[92:95], a[12:15], v[176:179]\n"
    "ds_read_b128 a[64:67], v224 offset:8192\n"
    "ds_read_b128 a[68:71], v225 offset:8192\n"
    "s_nop 0\n"
    "v_mfma_f32_16x16x32_bf16 v[180:183], a[80:83], a[16:19], v[148:151]\n"
    "v_exp_f32 v169, v169\n"
    "v_mfma_f32_16x16x32_bf16 v[180:183], a[84:87], a[20:23], v[180:183]\n"
    "v_cvt_pk_bf16_f32 v200, v168, v169\n"
    "s_max_i32 s80, s80, 70\n"
    "s_memtime s[82:83]\n"
    "s_nop 0\n"
    "v_mfma_f32_16x16x32_bf16 v[180:183], a[88:91], a[24:27], v[180:183]\n"
    "v_exp_f32 v170, v170\n"
    "v_mfma_f32_16x16x32_bf16 v[180:183], a[92:95], a[28:31], v[180:183]\n"
    "ds_read_b128 a[72:75], v226 offset:8192\n"
    "ds_read_b128 a[76:79], v227 offset:8192\n"
    "s_memrealtime s[84:85]\n"
    "s_nop 0\n"
    "v_mfma_f32_16x16x32_bf16 v[184:187], a[80:83], a[32:35], v[152:155]\n"
    "v_exp_f32 v171, v171\n"
    "v_mfma_f32_16x16x32_bf16 v[184:187], a[84:87], a[36:39], v[184:187]\n"
    "v_cvt_pk_bf16_f32 v201, v170, v171\n"
    "s_memrealtime s[76:77]\n"
    "s_nop 0\n"
    "v_mfma_f32_16x16x32_bf16 v[184:187], a[88:91], a[40:43], v[184:187]\n"
    "v_exp_f32 v172, v172\n"
    "v_mfma_f32_16x16x32_bf16 v[184:187], a[92:95], a[44:47], v[184:187]\n"
    "buffer_load_dwordx4 v240, s[44:47], s56 offen offset:2048 nt lds\n"
    "s_nop 0\n"
    "v_mfma_f32_16x16x32_bf16 v[188:191], a[80:83], a[48:51], v[156:159]\n"
    "v_exp_f32 v173, v173\n"
    "v_mfma_f32_16x16x32_bf16 v[188:191], a[84:87], a[52:55], v[188:191]\n"
    "v_cvt_pk_bf16_f32 v204, v172, v173\n"
    "s_nop 0\n"
    "v_mfma_f32_16x16x32_bf16 v[188:191], a[88:91], a[56:59], v[188:191]\n"
    "v_exp_f32 v174, v174\n"
    "v_mfma_f32_16x16x32_bf16 v[188:191], a[92:95], a[60:63], v[188:191]\n"
    "s_waitcnt lgkmcnt(0)\n"
    "s_sub_u32 s79, s78, s76\n"
    "s_cmp_gt_i32 s79, 0\n"
    "s_cbranch_scc1 Lpw17_%=\n"
    "Lpw17_%=b:\n"
    "s_max_i32 s79, s79, -12\n"
    "s_add_u32 s78, s76, s79\n"
    "s_lshr_b32 s79, s80, 1\n"
    "s_add_u32 s78, s78, s79\n"
    "s_nop 0\n"
    "v_mfma_f32_16x16x32_bf16 v[16:19], a[112:115], v[208:211], v[16:19]\n"
    "v_exp_f32 v175, v175\n"
    "v_mfma_f32_16x16x32_bf16 v[20:23], a[116:119], v[208:211], v[20:23]\n"
    "buffer_load_dwordx4 v241, s[44:47], s56 offen offset:3072 nt lds\n"
    "v_cvt_pk_bf16_f32 v205, v174, v175\n"
    "s_add_u32 s56, s56, 16384\n"
    "s_sub_u32 s86, s82, s86\n"
    "s_nop 0\n"
    "v_mfma_f32_16x16x32_bf16 v[24:27], a[120:123], v[208:211], v[24:27]\n"
    "v_exp_f32 v176, v176\n"
    "v_mfma_f32_16x16x32_bf16 v[28:31], a[124:127], v[208:211], v[28:31]\n"
    "s_waitcnt vmcnt(12)\n"
    "s_barrier\n"
    "s_nop 0\n"
    "v_mfma_f32_16x16x32_bf16 v[48:51], a[112:115], v[212:215], v[48:51]\n"
    "v_exp_f32 v177, v177\n"
    "v_mfma_f32_16x16x32_bf16 v[52:55], a[116:119], v[212:215], v[52:55]\n"
    "v_cvt_pk_bf16_f32 v194, v176, v177\n"
    "s_sub_u32 s87, s84, s87\n"
    "s_nop 0\n"
    "v_mfma_f32_16x16x32_bf16 v[56:59], a[120:123], v[212:215], v[56:59]\n"
    "v_exp_f32 v178, v178\n"
    "v_mfma_f32_16x16x32_bf16 v[60:63], a[124:127], v[212:215], v[60:63]\n"
    "ds_read_b64_tr_b16 a[96:97], v228\n"
    "ds_read_b64_tr_b16 a[98:99], v228 offset:4096\n"
    "s_nop 0\n"
    "v_mfma_f32_16x16x32_bf16 v[80:83], a[112:115], v[216:219], v[80:83]\n"
    "v_exp_f32 v179, v179\n"
    "v_mfma_f32_16x16x32_bf16 v[84:87], a[116:119], v[216:219], v[84:87]\n"
    "v_cvt_pk_bf16_f32 v195, v178, v179\n"
    "s_lshl_b32 s86, s86, 1\n"
    "s_mul_i32 s87, s87, 32\n"
    "s_nop 0\n"
    "v_mfma_f32_16x16x32_bf16 v[88:91], a[120:123], v[216:219], v[88:91]\n"
    "v_exp_f32 v180, v180\n"
    "v_mfma_f32_16x16x32_bf16 v[92:95], a[124:127], v[216:219], v[92:95]\n"
    "ds_read_b64_tr_b16 a[100:101], v229\n"
    "ds_read_b64_tr_b16 a[102:103], v229 offset:4096\n"
    "s_nop 0\n"
    "v_mfma_f32_16x16x32_bf16 v[112:115], a[112:115], v[220:223], v[112:115]\n"
    "v_exp_f32 v181, v181\n"
    "v_mfma_f32_16x16x32_bf16 v[116:119], a[116:119], v[220:223], v[116:119]\n"
    "v_cvt_pk_bf16_f32 v198, v180, v181\n"
    "s_cmp_gt_u32 s86, s87\n"
    "s_cselect_b32 s88, 1, 0\n"
    "s_nop 0\n"
    "v_mfma_f32_16x16x32_bf16 v[120:123], a[120:123], v[220:223], v[120:123]\n"
    "v_exp_f32 v182, v182\n"
    "v_mfma_f32_16x16x32_bf16 v[124:127], a[124:127], v[220:223], v[124:127]\n"
    "ds_read_b64_tr_b16 a[104:105], v230\n"
    "ds_read_b64_tr_b16 a[106:107], v230 offset:4096\n"
    "s_nop 0\n"
    "v_mfma_f32_16x16x32_bf16 v[128:131], a[128:131], v[208:211], v[128:131]\n"
    "v_exp_f32 v183, v183\n"
    "v_mfma_f32_16x16x32_bf16 v[132:135], a[128:131], v[212:215], v[132:135]\n"
    "v_cvt_pk_bf16_f32 v199, v182, v183\n"
    "s_mov_b32 s86, s82\n"
    "s_mov_b32 s87, s84\n"
    "s_nop 0\n"
    "v_mfma_f32_16x16x32_bf16 v[136:139], a[128:131], v[216:219], v[136:139]\n"
    "v_exp_f32 v184, v184\n"
    "v_mfma_f32_16x16x32_bf16 v[140:143], a[128:131], v[220:223], v[140:143]\n"
    "ds_read_b64_tr_b16 a[108:109], v231\n"
    "ds_read_b64_tr_b16 a[110:111], v231 offset:4096\n"
    "s_nop 0\n"
    "s_sub_u32 s58, s58, 1\n"
    "s_cmp_eq_u32 s88, 1\n"
    "s_cbranch_scc1 Lslow_%=\n"
    "Lfast_%=:\n"
    "v_mfma_f32_16x16x32_bf16 v[160:163], a[64:67], a[0:3], v[144:147]\n"
    "v_exp_f32 v185, v185\n"
    "v_mfma_f32_16x16x32_bf16 v[160:163], a[68:71], a[4:7], v[160:163]\n"
    "v_cvt_pk_bf16_f32 v202, v184, v185\n"
    "v_mfma_f32_16x16x32_bf16 v[160:163], a[72:75], a[8:11], v[160:163]\n"
    "v_exp_f32 v186, v186\n"
    "v_mfma_f32_16x16x32_bf16 v[160:163], a[76:79], a[12:15], v[160:163]\n"
    "ds_read_b128 a[80:83], v224 offset:12288\n"
    "ds_read_b128 a[84:87], v225 offset:12288\n"
    "v_mfma_f32_16x16x32_bf16 v[164:167], a[64:67], a[16:19], v[148:151]\n"
    "v_exp_f32 v187, v187\n"
    "v_mfma_f32_16x16x32_bf16 v[164:167], a[68:71], a[20:23], v[164:167]\n"
    "v_cvt_pk_bf16_f32 v203, v186, v187\n"
    "v_mfma_f32_16x16x32_bf16 v[164:167], a[72:75], a[24:27], v[164:167]\n"
    "v_exp_f32 v188, v188\n"
    "v_mfma_f32_16x16x32_bf16 v[164:167], a[76:79], a[28:31], v[164:167]\n"
    "ds_read_b128 a[88:91], v226 offset:12288\n"
    "ds_read_b128 a[92:95], v227 offset:12288\n"
    "v_mfma_f32_16x16x32_bf16 v[168:171], a[64:67], a[32:35], v[152:155]\n"
    "v_exp_f32 v189, v189\n"
    "s_memrealtime s[76:77]\n"
    "v_mfma_f32_16x16x32_bf16 v[168:171], a[68:71], a[36:39], v[168:171]\n"
    "v_cvt_pk_bf16_f32 v206, v188, v189\n"
    "s_add_u32 m0, s57, 49152\n"
    "v_mfma_f32_16x16x32_bf16 v[168:171], a[72:75], a[40:43], v[168:171]\n"
    "v_exp_f32 v190, v190\n"
    "v_mfma_f32_16x16x32_bf16 v[168:171], a[76:79], a[44:47], v[168:171]\n"
    "buffer_load_dwordx4 v236, s[40:43], s56 offen nt lds\n"
    "v_mfma_f32_16x16x32_bf16 v[172:175], a[64:67], a[48:51], v[156:159]\n"
    "v_exp_f32 v191, v191\n"
    "v_mfma_f32_16x16x32_bf16 v[172:175], a[68:71], a[52:55], v[172:175]\n"
    "v_cvt_pk_bf16_f32 v207, v190, v191\n"
    "v_mfma_f32_16x16x32_bf16 v[172:175], a[72:75], a[56:59], v[172:175]\n"
    "s_waitcnt lgkmcnt(0)\n"
    "s_sub_u32 s79, s78, s76\n"
    "v_mfma_f32_16x16x32_bf16 v[172:175], a[76:79], a[60:63], v[172:175]\n"
    "s_cmp_gt_i32 s79, 0\n"
    "s_cbranch_scc1 Lpw19_%=\n"
    "Lpw19_%=b:\n"
    "v_mfma_f32_16x16x32_bf16 v[0:3], a[96:99], v[192:195], v[0:3]\n"
    "s_max_i32 s79, s79, -12\n"
    "s_add_u32 s78, s76, s79\n"
    "v_mfma_f32_16x16x32_bf16 v[4:7], a[100:103], v[192:195], v[4:7]\n"
    "buffer_load_dwordx4 v237, s[40:43], s56 offen offset:1024 nt lds\n"
    "v_mfma_f32_16x16x32_bf16 v[8:11], a[104:107], v[192:195], v[8:11]\n"
    "v_exp_f32 v160, v160\n"
    "v_mfma_f32_16x16x32_bf16 v[12:15], a[108:111], v[192:195], v[12:15]\n"
    "ds_read_b64_tr_b16 a[112:113], v232\n"
    "ds_read_b64_tr_b16 a[114:115], v232 offset:4096\n"
    "v_mfma_f32_16x16x32_bf16 v[32:35], a[96:99], v[196:199], v[32:35]\n"
    "v_exp_f32 v161, v161\n"
    "v_mfma_f32_16x16x32_bf16 v[36:39], a[100:103], v[196:199], v[36:39]\n"
    "v_cvt_pk_bf16_f32 v208, v160, v161\n"
    "v_mfma_f32_16x16x32_bf16 v[40:43], a[104:107], v[196:199], v[40:43]\n"
    "v_exp_f32 v162, v162\n"
    "v_mfma_f32_16x16x32_bf16 v[44:47], a[108:111], v[196:199], v[44:47]\n"
    "ds_read_b64_tr_b16 a[116:117], v233\n"
    "ds_read_b64_tr_b16 a[118:119], v233 offset:4096\n"
    "v_mfma_f32_16x16x32_bf16 v[64:67], a[96:99], v[200:203], v[64:67]\n"
    "v_exp_f32 v163, v163\n"
    "v_mfma_f32_16x16x32_bf16 v[68:71], a[100:103], v[200:203], v[68:71]\n"
    "v_cvt_pk_bf16_f32 v209, v162, v163\n"
    "v_mfma_f32_16x16x32_bf16 v[72:75], a[104:107], v[200:203], v[72:75]\n"
    "v_exp_f32 v164, v164\n"
    "v_mfma_f32_16x16x32_bf16 v[76:79], a[108:111], v[200:203], v[76:79]\n"
    "ds_read_b64_tr_b16 a[120:121], v234\n"
    "ds_read_b64_tr_b16 a[122:123], v234 offset:4096\n"
    "v_mfma_f32_16x16x32_bf16 v[96:99], a[96:99], v[204:207], v[96:99]\n"
    "v_exp_f32 v165, v165\n"
    "v_mfma_f32_16x16x32_bf16 v[100:103], a[100:103], v[204:207], v[100:103]\n"
    "v_cvt_pk_bf16_f32 v212, v164, v165\n"
    "s_add_u32 s78, s78, s80\n"
    "v_mfma_f32_16x16x32_bf16 v[104:107], a[104:107], v[204:207], v[104:107]\n"
    "v_exp_f32 v166, v166\n"
    "v_mfma_f32_16x16x32_bf16 v[108:111], a[108:111], v[204:207], v[108:111]\n"
    "ds_read_b64_tr_b16 a[124:125], v235\n"
    "ds_read_b64_tr_b16 a[126:127], v235 offset:4096\n"
    "v_mfma_f32_16x16x32_bf16 v[176:179], a[80:83], a[0:3], v[144:147]\n"
    "v_exp_f32 v167, v167\n"
    "v_mfma_f32_16x16x32_bf16 v[176:179], a[84:87], a[4:7], v[176:179]\n"
    "v_cvt_pk_bf16_f32 v213, v166, v167\n"
    "s_cmp_eq_u32 s58, 8\n"
    "s_cbranch_scc1 Lb0_18_%=\n"
    "Lb0_18_%=b:\n"
    "s_sub_u32 s80, s80, 4\n"
    "v_mfma_f32_16x16x32_bf16 v[176:179], a[88:91], a[8:11], v[176:179]\n"
    "v_exp_f32 v168, v168\n"
    "v_mfma_f32_16x16x32_bf16 v[176:179], a[92:95], a[12:15], v[176:179]\n"
    "ds_read_b128 a[64:67], v224 offset:16384\n"
    "ds_read_b128 a[68:71], v225 offset:16384\n"
    "v_mfma_f32_16x16x32_bf16 v[180:183], a[80:83], a[16:19], v[148:151]\n"
    "v_exp_f32 v169, v169\n"
    "v_mfma_f32_16x16x32_bf16 v[180:183], a[84:87], a[20:23], v[180:183]\n"
    "v_cvt_pk_bf16_f32 v216, v168, v169\n"
    "s_max_i32 s80, s80, 70\n"
    "v_mfma_f32_16x16x32_bf16 v[180:183], a[88:91], a[24:27], v[180:183]\n"
    "v_exp_f32 v170, v170\n"
    "v_mfma_f32_16x16x32_bf16 v[180:183], a[92:95], a[28:31], v[180:183]\n"
    "ds_read_b128 a[72:75], v226 offset:16384\n"
    "ds_read_b128 a[76:79], v227 offset:16384\n"
    "v_mfma_f32_16x16x32_bf16 v[184:187], a[80:83], a[32:35], v[152:155]\n"
    "v_exp_f32 v171, v171\n"
    "v_mfma_f32_16x16x32_bf16 v[184:187], a[84:87], a[36:39], v[184:187]\n"
    "v_cvt_pk_bf16_f32 v217, v170, v171\n"
    "v_mfma_f32_16x16x32_bf16 v[184:187], a[88:91], a[40:43], v[184:187]\n"
    "v_exp_f32 v172, v172\n"
    "v_mfma_f32_16x16x32_bf16 v[184:187], a[92:95], a[44:47], v[184:187]\n"
    "buffer_load_dwordx4 v238, s[40:43], s56 offen offset:2048 nt lds\n"
    "v_mfma_f32_16x16x32_bf16 v[188:191], a[80:83], a[48:51], v[156:159]\n"
    "v_exp_f32 v173, v173\n"
    "v_mfma_f32_16x16x32_bf16 v[188:191], a[84:87], a[52:55], v[188:191]\n"
    "v_cvt_pk_bf16_f32 v220, v172, v173\n"
    "v_mfma_f32_16x16x32_bf16 v[188:191], a[88:91], a[56:59], v[188:191]\n"
    "v_exp_f32 v174, v174\n"
    "v_mfma_f32_16x16x32_bf16 v[188:191], a[92:95], a[60:63], v[188:191]\n"
    "s_waitcnt lgkmcnt(0)\n"
    "v_mfma_f32_16x16x32_bf16 v[16:19], a[112:115], v[192:195], v[16:19]\n"
    "v_exp_f32 v175, v175\n"
    "v_mfma_f32_16x16x32_bf16 v[20:23], a[116:119], v[192:195], v[20:23]\n"
    "buffer_load_dwordx4 v239, s[40:43], s56 offen offset:3072 nt lds\n"
    "v_cvt_pk_bf16_f32 v221, v174, v175\n"
    "v_mfma_f32_16x16x32_bf16 v[24:27], a[120:123], v[192:195], v[24:27]\n"
    "v_exp_f32 v176, v176\n"
    "v_mfma_f32_16x16x32_bf16 v[28:31], a[124:127], v[192:195], v[28:31]\n"
    "v_mfma_f32_16x16x32_bf16 v[48:51], a[112:115], v[196:199], v[48:51]\n"
    "v_exp_f32 v177, v177\n"
    "v_mfma_f32_16x16x32_bf16 v[52:55], a[116:119], v[196:199], v[52:55]\n"
    "v_cvt_pk_bf16_f32 v210, v176, v177\n"
    "v_mfma_f32_16x16x32_bf16 v[56:59], a[120:123], v[196:199], v[56:59]\n"
    "v_exp_f32 v178, v178\n"
    "v_mfma_f32_16x16x32_bf16 v[60:63], a[124:127], v[196:199], v[60:63]\n"
    "ds_read_b64_tr_b16 a[96:97], v228 offset:8192\n"
    "ds_read_b64_tr_b16 a[98:99], v228 offset:12288\n"
    "v_mfma_f32_16x16x32_bf16 v[80:83], a[112:115], v[200:203], v[80:83]\n"
    "v_exp_f32 v179, v179\n"
    "v_mfma_f32_16x16x32_bf16 v[84:87], a[116:119], v[200:203], v[84:87]\n"
    "v_cvt_pk_bf16_f32 v211, v178, v179\n"
    "v_mfma_f32_16x16x32_bf16 v[88:91], a[120:123], v[200:203], v[88:91]\n"
    "v_exp_f32 v180, v180\n"
    "v_mfma_f32_16x16x32_bf16 v[92:95], a[124:127], v[200:203], v[92:95]\n"
    "ds_read_b64_tr_b16 a[100:101], v229 offset:8192\n"
    "ds_read_b64_tr_b16 a[102:103], v229 offset:12288\n"
    "v_mfma_f32_16x16x32_bf16 v[112:115], a[112:115], v[204:207], v[112:115]\n"
    "v_exp_f32 v181, v181\n"
    "v_mfma_f32_16x16x32_bf16 v[116:119], a[116:119], v[204:207], v[116:119]\n"
    "v_cvt_pk_bf16_f32 v214, v180, v181\n"
    "v_mfma_f32_16x16x32_bf16 v[120:123], a[120:123], v[204:207], v[120:123]\n"
    "v_exp_f32 v182, v182\n"
    "v_mfma_f32_16x16x32_bf16 v[124:127], a[124:127], v[204:207], v[124:127]\n"
    "ds_read_b64_tr_b16 a[104:105], v230 offset:8192\n"
    "ds_read_b64_tr_b16 a[106:107], v230 offset:12288\n"
    "v_mfma_f32_16x16x32_bf16 v[128:131], a[128:131], v[192:195], v[128:131]\n"
    "v_exp_f32 v183, v183\n"
    "v_mfma_f32_16x16x32_bf16 v[132:135], a[128:131], v[196:199], v[132:135]\n"
    "v_cvt_pk_bf16_f32 v215, v182, v183\n"
    "v_mfma_f32_16x16x32_bf16 v[136:139], a[128:131], v[200:203], v[136:139]\n"
    "v_exp_f32 v184, v184\n"
    "v_mfma_f32_16x16x32_bf16 v[140:143], a[128:131], v[204:207], v[140:143]\n"
    "ds_read_b64_tr_b16 a[108:109], v231 offset:8192\n"
    "ds_read_b64_tr_b16 a[110:111], v231 offset:12288\n"
    "v_mfma_f32_16x16x32_bf16 v[160:163], a[64:67], a[0:3], v[144:147]\n"
    "v_exp_f32 v185, v185\n"
    "v_mfma_f32_16x16x32_bf16 v[160:163], a[68:71], a[4:7], v[160:163]\n"
    "v_cvt_pk_bf16_f32 v218, v184, v185\n"
    "v_mfma_f32_16x16x32_bf16 v[160:163], a[72:75], a[8:11], v[160:163]\n"
    "v_exp_f32 v186, v186\n"
    "v_mfma_f32_16x16x32_bf16 v[160:163], a[76:79], a[12:15], v[160:163]\n"
    "ds_read_b128 a[80:83], v224 offset:20480\n"
    "ds_read_b128 a[84:87], v225 offset:20480\n"
    "v_mfma_f32_16x16x32_bf16 v[164:167], a[64:67], a[16:19], v[148:151]\n"
    "v_exp_f32 v187, v187\n"
    "v_mfma_f32_16x16x32_bf16 v[164:167], a[68:71], a[20:23], v[164:167]\n"
    "v_cvt_pk_bf16_f32 v219, v186, v187\n"
    "v_mfma_f32_16x16x32_bf16 v[164:167], a[72:75], a[24:27], v[164:167]\n"
    "v_exp_f32 v188, v188\n"
    "v_mfma_f32_16x16x32_bf16 v[164:167], a[76:79], a[28:31], v[164:167]\n"
    "ds_read_b128 a[88:91], v226 offset:20480\n"
    "ds_read_b128 a[92:95], v227 offset:20480\n"
    "v_mfma_f32_16x16x32_bf16 v[168:171], a[64:67], a[32:35], v[152:155]\n"
    "v_exp_f32 v189, v189\n"
    "s_memrealtime s[76:77]\n"
    "v_mfma_f32_16x16x32_bf16 v[168:171], a[68:71], a[36:39], v[168:171]\n"
    "v_cvt_pk_bf16_f32 v222, v188, v189\n"
    "s_add_u32 m0, s57, 114688\n"
    "v_mfma_f32_16x16x32_bf16 v[168:171], a[72:75], a[40:43], v[168:171]\n"
    "v_exp_f32 v190, v190\n"
    "v_mfma_f32_16x16x32_bf16 v[168:171], a[76:79], a[44:47], v[168:171]\n"
    "buffer_load_dwordx4 v240, s[44:47], s56 offen nt lds\n"
    "v_mfma_f32_16x16x32_bf16 v[172:175], a[64:67], a[48:51], v[156:159]\n"
    "v_exp_f32 v191, v191\n"
    "v_mfma_f32_16x16x32_bf16 v[172:175], a[68:71], a[52:55], v[172:175]\n"
    "v_cvt_pk_bf16_f32 v223, v190, v191\n"
    "v_mfma_f32_16x16x32_bf16 v[172:175], a[72:75], a[56:59], v[172:175]\n"
    "s_waitcnt lgkmcnt(0)\n"
    "s_sub_u32 s79, s78, s76\n"
    "v_mfma_f32_16x16x32_bf16 v[172:175], a[76:79], a[60:63], v[172:175]\n"
    "s_cmp_gt_i32 s79, 0\n"
    "s_cbranch_scc1 Lpw20_%=\n"
    "Lpw20_%=b:\n"
    "v_mfma_f32_16x16x32_bf16 v[0:3], a[96:99], v[208:211], v[0:3]\n"
    "s_max_i32 s79, s79, -12\n"
    "s_add_u32 s78, s76, s79\n"
    "v_mfma_f32_16x16x32_bf16 v[4:7], a[100:103], v[208:211], v[4:7]\n"
    "buffer_load_dwordx4 v241, s[44:47], s56 offen offset:1024 nt lds\n"
    "v_mfma_f32_16x16x32_bf16 v[8:11], a[104:107], v[208:211], v[8:11]\n"
    "v_exp_f32 v160, v160\n"
    "v_mfma_f32_16x16x32_bf16 v[12:15], a[108:111], v[208:211], v[12:15]\n"
    "ds_read_b64_tr_b16 a[112:113], v232 offset:8192\n"
    "ds_read_b64_tr_b16 a[114:115], v232 offset:12288\n"
    "v_mfma_f32_16x16x32_bf16 v[32:35], a[96:99], v[212:215], v[32:35]\n"
    "v_exp_f32 v161, v161\n"
    "v_mfma_f32_16x16x32_bf16 v[36:39], a[100:103], v[212:215], v[36:39]\n"
    "v_cvt_pk_bf16_f32 v192, v160, v161\n"
    "v_mfma_f32_16x16x32_bf16 v[40:43], a[104:107], v[212:215], v[40:43]\n"
    "v_exp_f32 v162, v162\n"
    "v_mfma_f32_16x16x32_bf16 v[44:47], a[108:111], v[212:215], v[44:47]\n"
    "ds_read_b64_tr_b16 a[116:117], v233 offset:8192\n"
    "ds_read_b64_tr_b16 a[118:119], v233 offset:12288\n"
    "v_mfma_f32_16x16x32_bf16 v[64:67], a[96:99], v[216:219], v[64:67]\n"
    "v_exp_f32 v163, v163\n"
    "v_mfma_f32_16x16x32_bf16 v[68:71], a[100:103], v[216:219], v[68:71]\n"
    "v_cvt_pk_bf16_f32 v193, v162, v163\n"
    "v_mfma_f32_16x16x32_bf16 v[72:75], a[104:107], v[216:219], v[72:75]\n"
    "v_exp_f32 v164, v164\n"
    "v_mfma_f32_16x16x32_bf16 v[76:79], a[108:111], v[216:219], v[76:79]\n"
    "ds_read_b64_tr_b16 a[120:121], v234 offset:8192\n"
    "ds_read_b64_tr_b16 a[122:123], v234 offset:12288\n"
    "v_mfma_f32_16x16x32_bf16 v[96:99], a[96:99], v[220:223], v[96:99]\n"
    "v_exp_f32 v165, v165\n"
    "v_mfma_f32_16x16x32_bf16 v[100:103], a[100:103], v[220:223], v[100:103]\n"
    "v_cvt_pk_bf16_f32 v196, v164, v165\n"
    "s_add_u32 s78, s78, s80\n"
    "v_mfma_f32_16x16x32_bf16 v[104:107], a[104:107], v[220:223], v[104:107]\n"
    "v_exp_f32 v166, v166\n"
    "v_mfma_f32_16x16x32_bf16 v[108:111], a[108:111], v[220:223], v[108:111]\n"
    "ds_read_b64_tr_b16 a[124:125], v235 offset:8192\n"
    "ds_read_b64_tr_b16 a[126:127], v235 offset:12288\n"
    "v_mfma_f32_16x16x32_bf16 v[176:179], a[80:83], a[0:3], v[144:147]\n"
    "v_exp_f32 v167, v167\n"
    "v_mfma_f32_16x16x32_bf16 v[176:179], a[84:87], a[4:7], v[176:179]\n"
    "v_cvt_pk_bf16_f32 v197, v166, v167\n"
    "s_sub_u32 s80, s80, 4\n"
    "v_mfma_f32_16x16x32_bf16 v[176:179], a[88:91], a[8:11], v[176:179]\n"
    "v_exp_f32 v168, v168\n"
    "v_mfma_f32_16x16x32_bf16 v[176:179], a[92:95], a[12:15], v[176:179]\n"
    "ds_read_b128 a[64:67], v224 offset:24576\n"
    "ds_read_b128 a[68:71], v225 offset:24576\n"
    "v_mfma_f32_16x16x32_bf16 v[180:183], a[80:83], a[16:19], v[148:151]\n"
    "v_exp_f32 v169, v169\n"
    "v_mfma_f32_16x16x32_bf16 v[180:183], a[84:87], a[20:23], v[180:183]\n"
    "v_cvt_pk_bf16_f32 v200, v168, v169\n"
    "s_max_i32 s80, s80, 70\n"
    "v_mfma_f32_16x16x32_bf16 v[180:183], a[88:91], a[24:27], v[180:183]\n"
    "v_exp_f32 v170, v170\n"
    "v_mfma_f32_16x16x32_bf16 v[180:183], a[92:95], a[28:31], v[180:183]\n"
    "ds_read_b128 a[72:75], v226 offset:24576\n"
    "ds_read_b128 a[76:79], v227 offset:24576\n"
    "v_mfma_f32_16x16x32_bf16 v[184:187], a[80:83], a[32:35], v[152:155]\n"
    "v_exp_f32 v171, v171\n"
    "v_mfma_f32_16x16x32_bf16 v[184:187], a[84:87], a[36:39], v[184:187]\n"
    "v_cvt_pk_bf16_f32 v201, v170, v171\n"
    "v_mfma_f32_16x16x32_bf16 v[184:187], a[88:91], a[40:43], v[184:187]\n"
    "v_exp_f32 v172, v172\n"
    "v_mfma_f32_16x16x32_bf16 v[184:187], a[92:95], a[44:47], v[184:187]\n"
    "buffer_load_dwordx4 v240, s[44:47], s56 offen offset:2048 nt lds\n"
    "v_mfma_f32_16x16x32_bf16 v[188:191], a[80:83], a[48:51], v[156:159]\n"
    "v_exp_f32 v173, v173\n"
    "v_mfma_f32_16x16x32_bf16 v[188:191], a[84:87], a[52:55], v[188:191]\n"
    "v_cvt_pk_bf16_f32 v204, v172, v173\n"
    "v_mfma_f32_16x16x32_bf16 v[188:191], a[88:91], a[56:59], v[188:191]\n"
    "v_exp_f32 v174, v174\n"
    "v_mfma_f32_16x16x32_bf16 v[188:191], a[92:95], a[60:63], v[188:191]\n"
    "s_waitcnt lgkmcnt(0)\n"
    "v_mfma_f32_16x16x32_bf16 v[16:19], a[112:115], v[208:211], v[16:19]\n"
    "v_exp_f32 v175, v175\n"
    "v_mfma_f32_16x16x32_bf16 v[20:23], a[116:119], v[208:211], v[20:23]\n"
    "buffer_load_dwordx4 v241, s[44:47], s56 offen offset:3072 nt lds\n"
    "v_cvt_pk_bf16_f32 v205, v174, v175\n"
    "s_add_u32 s56, s56, 16384\n"
    "v_mfma_f32_16x16x32_bf16 v[24:27], a[120:123], v[208:211], v[24:27]\n"
    "v_exp_f32 v176, v176\n"
    "v_mfma_f32_16x16x32_bf16 v[28:31], a[124:127], v[208:211], v[28:31]\n"
    "s_waitcnt vmcnt(12)\n"
    "s_barrier\n"
    "v_mfma_f32_16x16x32_bf16 v[48:51], a[112:115], v[212:215], v[48:51]\n"
    "v_exp_f32 v177, v177\n"
    "v_mfma_f32_16x16x32_bf16 v[52:55], a[116:119], v[212:215], v[52:55]\n"
    "v_cvt_pk_bf16_f32 v194, v176, v177\n"
    "v_mfma_f32_16x16x32_bf16 v[56:59], a[120:123], v[212:215], v[56:59]\n"
    "v_exp_f32 v178, v178\n"
    "v_mfma_f32_16x16x32_bf16 v[60:63], a[124:127], v[212:215], v[60:63]\n"
    "ds_read_b64_tr_b16 a[96:97], v228 offset:16384\n"
    "ds_read_b64_tr_b16 a[98:99], v228 offset:20480\n"
    "v_mfma_f32_16x16x32_bf16 v[80:83], a[112:115], v[216:219], v[80:83]\n"
    "v_exp_f32 v179, v179\n"
    "v_mfma_f32_16x16x32_bf16 v[84:87], a[116:119], v[216:219], v[84:87]\n"
    "v_cvt_pk_bf16_f32 v195, v178, v179\n"
    "v_mfma_f32_16x16x32_bf16 v[88:91], a[120:123], v[216:219], v[88:91]\n"
    "v_exp_f32 v180, v180\n"
    "v_mfma_f32_16x16x32_bf16 v[92:95], a[124:127], v[216:219], v[92:95]\n"
    "ds_read_b64_tr_b16 a[100:101], v229 offset:16384\n"
    "ds_read_b64_tr_b16 a[102:103], v229 offset:20480\n"
    "v_mfma_f32_16x16x32_bf16 v[112:115], a[112:115], v[220:223], v[112:115]\n"
    "v_exp_f32 v181, v181\n"
    "v_mfma_f32_16x16x32_bf16 v[116:119], a[116:119], v[220:223], v[116:119]\n"
    "v_cvt_pk_bf16_f32 v198, v180, v181\n"
    "v_mfma_f32_16x16x32_bf16 v[120:123], a[120:123], v[220:223], v[120:123]\n"
    "v_exp_f32 v182, v182\n"
    "v_mfma_f32_16x16x32_bf16 v[124:127], a[124:127], v[220:223], v[124:127]\n"
    "ds_read_b64_tr_b16 a[104:105], v230 offset:16384\n"
    "ds_read_b64_tr_b16 a[106:107], v230 offset:20480\n"
    "v_mfma_f32_16x16x32_bf16 v[128:131], a[128:131], v[208:211], v[128:131]\n"
    "v_exp_f32 v183, v183\n"
    "v_mfma_f32_16x16x32_bf16 v[132:135], a[128:131], v[212:215], v[132:135]\n"
    "v_cvt_pk_bf16_f32 v199, v182, v183\n"
    "v_mfma_f32_16x16x32_bf16 v[136:139], a[128:131], v[216:219], v[136:139]\n"
    "v_exp_f32 v184, v184\n"
    "v_mfma_f32_16x16x32_bf16 v[140:143], a[128:131], v[220:223], v[140:143]\n"
    "ds_read_b64_tr_b16 a[108:109], v231 offset:16384\n"
    "ds_read_b64_tr_b16 a[110:111], v231 offset:20480\n"
    "v_mfma_f32_16x16x32_bf16 v[160:163], a[64:67], a[0:3], v[144:147]\n"
    "v_exp_f32 v185, v185\n"
    "v_mfma_f32_16x16x32_bf16 v[160:163], a[68:71], a[4:7], v[160:163]\n"
    "v_cvt_pk_bf16_f32 v202, v184, v185\n"
    "v_mfma_f32_16x16x32_bf16 v[160:163], a[72:75], a[8:11], v[160:163]\n"
    "v_exp_f32 v186, v186\n"
    "v_mfma_f32_16x16x32_bf16 v[160:163], a[76:79], a[12:15], v[160:163]\n"
    "ds_read_b128 a[80:83], v224 offset:28672\n"
    "ds_read_b128 a[84:87], v225 offset:28672\n"
    "v_mfma_f32_16x16x32_bf16 v[164:167], a[64:67], a[16:19], v[148:151]\n"
    "v_exp_f32 v187, v187\n"
    "v_mfma_f32_16x16x32_bf16 v[164:167], a[68:71], a[20:23], v[164:167]\n"
    "v_cvt_pk_bf16_f32 v203, v186, v187\n"
    "v_mfma_f32_16x16x32_bf16 v[164:167], a[72:75], a[24:27], v[164:167]\n"
    "v_exp_f32 v188, v188\n"
    "v_mfma_f32_16x16x32_bf16 v[164:167], a[76:79], a[28:31], v[164:167]\n"
    "ds_read_b128 a[88:91], v226 offset:28672\n"
    "ds_read_b128 a[92:95], v227 offset:28672\n"
    "v_mfma_f32_16x16x32_bf16 v[168:171], a[64:67], a[32:35], v[152:155]\n"
    "v_exp_f32 v189, v189\n"
    "s_memrealtime s[76:77]\n"
    "v_mfma_f32_16x16x32_bf16 v[168:171], a[68:71], a[36:39], v[168:171]\n"
    "v_cvt_pk_bf16_f32 v206, v188, v189\n"
    "s_add_u32 m0, s57, 0\n"
    "v_mfma_f32_16x16x32_bf16 v[168:171], a[72:75], a[40:43], v[168:171]\n"
    "v_exp_f32 v190, v190\n"
    "v_mfma_f32_16x16x32_bf16 v[168:171], a[76:79], a[44:47], v[168:171]\n"
    "buffer_load_dwordx4 v236, s[40:43], s56 offen nt lds\n"
    "v_mfma_f32_16x16x32_bf16 v[172:175], a[64:67], a[48:51], v[156:159]\n"
    "v_exp_f32 v191, v191\n"
    "v_mfma_f32_16x16x32_bf16 v[172:175], a[68:71], a[52:55], v[172:175]\n"
    "v_cvt_pk_bf16_f32 v207, v190, v191\n"
    "v_mfma_f32_16x16x32_bf16 v[172:175], a[72:75], a[56:59], v[172:175]\n"
    "s_waitcnt lgkmcnt(0)\n"
    "s_sub_u32 s79, s78, s76\n"
    "v_mfma_f32_16x16x32_bf16 v[172:175], a[76:79], a[60:63], v[172:175]\n"
    "s_cmp_gt_i32 s79, 0\n"
    "s_cbranch_scc1 Lpw21_%=\n"
    "Lpw21_%=b:\n"
    "v_mfma_f32_16x16x32_bf16 v[0:3], a[96:99], v[192:195], v[0:3]\n"
    "s_max_i32 s79, s79, -12\n"
    "s_add_u32 s78, s76, s79\n"
    "v_mfma_f32_16x16x32_bf16 v[4:7], a[100:103], v[192:195], v[4:7]\n"
    "buffer_load_dwordx4 v237, s[40:43], s56 offen offset:1024 nt lds\n"
    "v_mfma_f32_16x16x32_bf16 v[8:11], a[104:107], v[192:195], v[8:11]\n"
    "v_exp_f32 v160, v160\n"
    "v_mfma_f32_16x16x32_bf16 v[12:15], a[108:111], v[192:195], v[12:15]\n"
    "ds_read_b64_tr_b16 a[112:113], v232 offset:16384\n"
    "ds_read_b64_tr_b16 a[114:115], v232 offset:20480\n"
    "v_mfma_f32_16x16x32_bf16 v[32:35], a[96:99], v[196:199], v[32:35]\n"
    "v_exp_f32 v161, v161\n"
    "v_mfma_f32_16x16x32_bf16 v[36:39], a[100:103], v[196:199], v[36:39]\n"
    "v_cvt_pk_bf16_f32 v208, v160, v161\n"
    "v_mfma_f32_16x16x32_bf16 v[40:43], a[104:107], v[196:199], v[40:43]\n"
    "v_exp_f32 v162, v162\n"
    "v_mfma_f32_16x16x32_bf16 v[44:47], a[108:111], v[196:199], v[44:47]\n"
    "ds_read_b64_tr_b16 a[116:117], v233 offset:16384\n"
    "ds_read_b64_tr_b16 a[118:119], v233 offset:20480\n"
    "v_mfma_f32_16x16x32_bf16 v[64:67], a[96:99], v[200:203], v[64:67]\n"
    "v_exp_f32 v163, v163\n"
    "v_mfma_f32_16x16x32_bf16 v[68:71], a[100:103], v[200:203], v[68:71]\n"
    "v_cvt_pk_bf16_f32 v209, v162, v163\n"
    "v_mfma_f32_16x16x32_bf16 v[72:75], a[104:107], v[200:203], v[72:75]\n"
    "v_exp_f32 v164, v164\n"
    "v_mfma_f32_16x16x32_bf16 v[76:79], a[108:111], v[200:203], v[76:79]\n"
    "ds_read_b64_tr_b16 a[120:121], v234 offset:16384\n"
    "ds_read_b64_tr_b16 a[122:123], v234 offset:20480\n"
    "v_mfma_f32_16x16x32_bf16 v[96:99], a[96:99], v[204:207], v[96:99]\n"
    "v_exp_f32 v165, v165\n"
    "v_mfma_f32_16x16x32_bf16 v[100:103], a[100:103], v[204:207], v[100:103]\n"
    "v_cvt_pk_bf16_f32 v212, v164, v165\n"
    "s_add_u32 s78, s78, s80\n"
    "v_mfma_f32_16x16x32_bf16 v[104:107], a[104:107], v[204:207], v[104:107]\n"
    "v_exp_f32 v166, v166\n"
    "v_mfma_f32_16x16x32_bf16 v[108:111], a[108:111], v[204:207], v[108:111]\n"
    "ds_read_b64_tr_b16 a[124:125], v235 offset:16384\n"
    "ds_read_b64_tr_b16 a[126:127], v235 offset:20480\n"
    "v_mfma_f32_16x16x32_bf16 v[176:179], a[80:83], a[0:3], v[144:147]\n"
    "v_exp_f32 v167, v167\n"
    "v_mfma_f32_16x16x32_bf16 v[176:179], a[84:87], a[4:7], v[176:179]\n"
    "v_cvt_pk_bf16_f32 v213, v166, v167\n"
    "s_sub_u32 s80, s80, 4\n"
    "v_mfma_f32_16x16x32_bf16 v[176:179], a[88:91], a[8:11], v[176:179]\n"
    "v_exp_f32 v168, v168\n"
    "v_mfma_f32_16x16x32_bf16 v[176:179], a[92:95], a[12:15], v[176:179]\n"
    "ds_read_b128 a[64:67], v224 offset:32768\n"
    "ds_read_b128 a[68:71], v225 offset:32768\n"
    "v_mfma_f32_16x16x32_bf16 v[180:183], a[80:83], a[16:19], v[148:151]\n"
    "v_exp_f32 v169, v169\n"
    "v_mfma_f32_16x16x32_bf16 v[180:183], a[84:87], a[20:23], v[180:183]\n"
    "v_cvt_pk_bf16_f32 v216, v168, v169\n"
    "s_max_i32 s80, s80, 70\n"
    "v_mfma_f32_16x16x32_bf16 v[180:183], a[88:91], a[24:27], v[180:183]\n"
    "v_exp_f32 v170, v170\n"
    "v_mfma_f32_16x16x32_bf16 v[180:183], a[92:95], a[28:31], v[180:183]\n"
    "ds_read_b128 a[72:75], v226 offset:32768\n"
    "ds_read_b128 a[76:79], v227 offset:32768\n"
    "v_mfma_f32_16x16x32_bf16 v[184:187], a[80:83], a[32:35], v[152:155]\n"
    "v_exp_f32 v171, v171\n"
    "v_mfma_f32_16x16x32_bf16 v[184:187], a[84:87], a[36:39], v[184:187]\n"
    "v_cvt_pk_bf16_f32 v217, v170, v171\n"
    "v_mfma_f32_16x16x32_bf16 v[184:187], a[88:91], a[40:43], v[184:187]\n"
    "v_exp_f32 v172, v172\n"
    "v_mfma_f32_16x16x32_bf16 v[184:187], a[92:95], a[44:47], v[184:187]\n"
    "buffer_load_dwordx4 v238, s[40:43], s56 offen offset:2048 nt lds\n"
    "v_mfma_f32_16x16x32_bf16 v[188:191], a[80:83], a[48:51], v[156:159]\n"
    "v_exp_f32 v173, v173\n"
    "v_mfma_f32_16x16x32_bf16 v[188:191], a[84:87], a[52:55], v[188:191]\n"
    "v_cvt_pk_bf16_f32 v220, v172, v173\n"
    "v_mfma_f32_16x16x32_bf16 v[188:191], a[88:91], a[56:59], v[188:191]\n"
    "v_exp_f32 v174, v174\n"
    "v_mfma_f32_16x16x32_bf16 v[188:191], a[92:95], a[60:63], v[188:191]\n"
    "s_waitcnt lgkmcnt(0)\n"
    "v_mfma_f32_16x16x32_bf16 v[16:19], a[112:115], v[192:195], v[16:19]\n"
    "v_exp_f32 v175, v175\n"
    "v_mfma_f32_16x16x32_bf16 v[20:23], a[116:119], v[192:195], v[20:23]\n"
    "buffer_load_dwordx4 v239, s[40:43], s56 offen offset:3072 nt lds\n"
    "v_cvt_pk_bf16_f32 v221, v174, v175\n"
    "v_mfma_f32_16x16x32_bf16 v[24:27], a[120:123], v[192:195], v[24:27]\n"
    "v_exp_f32 v176, v176\n"
    "v_mfma_f32_16x16x32_bf16 v[28:31], a[124:127], v[192:195], v[28:31]\n"
    "v_mfma_f32_16x16x32_bf16 v[48:51], a[112:115], v[196:199], v[48:51]\n"
    "v_exp_f32 v177, v177\n"
    "v_mfma_f32_16x16x32_bf16 v[52:55], a[116:119], v[196:199], v[52:55]\n"
    "v_cvt_pk_bf16_f32 v210, v176, v177\n"
    "v_mfma_f32_16x16x32_bf16 v[56:59], a[120:123], v[196:199], v[56:59]\n"
    "v_exp_f32 v178, v178\n"
    "v_mfma_f32_16x16x32_bf16 v[60:63], a[124:127], v[196:199], v[60:63]\n"
    "ds_read_b64_tr_b16 a[96:97], v228 offset:24576\n"
    "ds_read_b64_tr_b16 a[98:99], v228 offset:28672\n"
    "v_mfma_f32_16x16x32_bf16 v[80:83], a[112:115], v[200:203], v[80:83]\n"
    "v_exp_f32 v179, v179\n"
    "v_mfma_f32_16x16x32_bf16 v[84:87], a[116:119], v[200:203], v[84:87]\n"
    "v_cvt_pk_bf16_f32 v211, v178, v179\n"
    "v_mfma_f32_16x16x32_bf16 v[88:91], a[120:123], v[200:203], v[88:91]\n"
    "v_exp_f32 v180, v180\n"
    "v_mfma_f32_16x16x32_bf16 v[92:95], a[124:127], v[200:203], v[92:95]\n"
    "ds_read_b64_tr_b16 a[100:101], v229 offset:24576\n"
    "ds_read_b64_tr_b16 a[102:103], v229 offset:28672\n"
    "v_mfma_f32_16x16x32_bf16 v[112:115], a[112:115], v[204:207], v[112:115]\n"
    "v_exp_f32 v181, v181\n"
    "v_mfma_f32_16x16x32_bf16 v[116:119], a[116:119], v[204:207], v[116:119]\n"
    "v_cvt_pk_bf16_f32 v214, v180, v181\n"
    "v_mfma_f32_16x16x32_bf16 v[120:123], a[120:123], v[204:207], v[120:123]\n"
    "v_exp_f32 v182, v182\n"
    "v_mfma_f32_16x16x32_bf16 v[124:127], a[124:127], v[204:207], v[124:127]\n"
    "ds_read_b64_tr_b16 a[104:105], v230 offset:24576\n"
    "ds_read_b64_tr_b16 a[106:107], v230 offset:28672\n"
    "v_mfma_f32_16x16x32_bf16 v[128:131], a[128:131], v[192:195], v[128:131]\n"
    "v_exp_f32 v183, v183\n"
    "v_mfma_f32_16x16x32_bf16 v[132:135], a[128:131], v[196:199], v[132:135]\n"
    "v_cvt_pk_bf16_f32 v215, v182, v183\n"
    "v_mfma_f32_16x16x32_bf16 v[136:139], a[128:131], v[200:203], v[136:139]\n"
    "v_exp_f32 v184, v184\n"
    "v_mfma_f32_16x16x32_bf16 v[140:143], a[128:131], v[204:207], v[140:143]\n"
    "ds_read_b64_tr_b16 a[108:109], v231 offset:24576\n"
    "ds_read_b64_tr_b16 a[110:111], v231 offset:28672\n"
    "v_mfma_f32_16x16x32_bf16 v[160:163], a[64:67], a[0:3], v[144:147]\n"
    "v_exp_f32 v185, v185\n"
    "v_mfma_f32_16x16x32_bf16 v[160:163], a[68:71], a[4:7], v[160:163]\n"
    "v_cvt_pk_bf16_f32 v218, v184, v185\n"
    "v_mfma_f32_16x16x32_bf16 v[160:163], a[72:75], a[8:11], v[160:163]\n"
    "v_exp_f32 v186, v186\n"
    "v_mfma_f32_16x16x32_bf16 v[160:163], a[76:79], a[12:15], v[160:163]\n"
    "ds_read_b128 a[80:83], v224 offset:36864\n"
    "ds_read_b128 a[84:87], v225 offset:36864\n"
    "v_mfma_f32_16x16x32_bf16 v[164:167], a[64:67], a[16:19], v[148:151]\n"
    "v_exp_f32 v187, v187\n"
    "v_mfma_f32_16x16x32_bf16 v[164:167], a[68:71], a[20:23], v[164:167]\n"
    "v_cvt_pk_bf16_f32 v219, v186, v187\n"
    "v_mfma_f32_16x16x32_bf16 v[164:167], a[72:75], a[24:27], v[164:167]\n"
    "v_exp_f32 v188, v188\n"
    "v_mfma_f32_16x16x32_bf16 v[164:167], a[76:79], a[28:31], v[164:167]\n"
    "ds_read_b128 a[88:91], v226 offset:36864\n"
    "ds_read_b128 a[92:95], v227 offset:36864\n"
    "v_mfma_f32_16x16x32_bf16 v[168:171], a[64:67], a[32:35], v[152:155]\n"
    "v_exp_f32 v189, v189\n"
    "s_memrealtime s[76:77]\n"
    "v_mfma_f32_16x16x32_bf16 v[168:171], a[68:71], a[36:39], v[168:171]\n"
    "v_cvt_pk_bf16_f32 v222, v188, v189\n"
    "s_add_u32 m0, s57, 65536\n"
    "v_mfma_f32_16x16x32_bf16 v[168:171], a[72:75], a[40:43], v[168:171]\n"
    "v_exp_f32 v190, v190\n"
    "v_mfma_f32_16x16x32_bf16 v[168:171], a[76:79], a[44:47], v[168:171]\n"
    "buffer_load_dwordx4 v240, s[44:47], s56 offen nt lds\n"
    "v_mfma_f32_16x16x32_bf16 v[172:175], a[64:67], a[48:51], v[156:159]\n"
    "v_exp_f32 v191, v191\n"
    "v_mfma_f32_16x16x32_bf16 v[172:175], a[68:71], a[52:55], v[172:175]\n"
    "v_cvt_pk_bf16_f32 v223, v190, v191\n"
    "v_mfma_f32_16x16x32_bf16 v[172:175], a[72:75], a[56:59], v[172:175]\n"
    "s_waitcnt lgkmcnt(0)\n"
    "s_sub_u32 s79, s78, s76\n"
    "v_mfma_f32_16x16x32_bf16 v[172:175], a[76:79], a[60:63], v[172:175]\n"
    "s_cmp_gt_i32 s79, 0\n"
    "s_cbranch_scc1 Lpw22_%=\n"
    "Lpw22_%=b:\n"
    "v_mfma_f32_16x16x32_bf16 v[0:3], a[96:99], v[208:211], v[0:3]\n"
    "s_max_i32 s79, s79, -12\n"
    "s_add_u32 s78, s76, s79\n"
    "v_mfma_f32_16x16x32_bf16 v[4:7], a[100:103], v[208:211], v[4:7]\n"
    "buffer_load_dwordx4 v241, s[44:47], s56 offen offset:1024 nt lds\n"
    "v_mfma_f32_16x16x32_bf16 v[8:11], a[104:107], v[208:211], v[8:11]\n"
    "v_exp_f32 v160, v160\n"
    "v_mfma_f32_16x16x32_bf16 v[12:15], a[108:111], v[208:211], v[12:15]\n"
    "ds_read_b64_tr_b16 a[112:113], v232 offset:24576\n"
    "ds_read_b64_tr_b16 a[114:115], v232 offset:28672\n"
    "v_mfma_f32_16x16x32_bf16 v[32:35], a[96:99], v[212:215], v[32:35]\n"
    "v_exp_f32 v161, v161\n"
    "v_mfma_f32_16x16x32_bf16 v[36:39], a[100:103], v[212:215], v[36:39]\n"
    "v_cvt_pk_bf16_f32 v192, v160, v161\n"
    "v_mfma_f32_16x16x32_bf16 v[40:43], a[104:107], v[212:215], v[40:43]\n"
    "v_exp_f32 v162, v162\n"
    "v_mfma_f32_16x16x32_bf16 v[44:47], a[108:111], v[212:215], v[44:47]\n"
    "ds_read_b64_tr_b16 a[116:117], v233 offset:24576\n"
    "ds_read_b64_tr_b16 a[118:119], v233 offset:28672\n"
    "v_mfma_f32_16x16x32_bf16 v[64:67], a[96:99], v[216:219], v[64:67]\n"
    "v_exp_f32 v163, v163\n"
    "v_mfma_f32_16x16x32_bf16 v[68:71], a[100:103], v[216:219], v[68:71]\n"
    "v_cvt_pk_bf16_f32 v193, v162, v163\n"
    "v_mfma_f32_16x16x32_bf16 v[72:75], a[104:107], v[216:219], v[72:75]\n"
    "v_exp_f32 v164, v164\n"
    "v_mfma_f32_16x16x32_bf16 v[76:79], a[108:111], v[216:219], v[76:79]\n"
    "ds_read_b64_tr_b16 a[120:121], v234 offset:24576\n"
    "ds_read_b64_tr_b16 a[122:123], v234 offset:28672\n"
    "v_mfma_f32_16x16x32_bf16 v[96:99], a[96:99], v[220:223], v[96:99]\n"
    "v_exp_f32 v165, v165\n"
    "v_mfma_f32_16x16x32_bf16 v[100:103], a[100:103], v[220:223], v[100:103]\n"
    "v_cvt_pk_bf16_f32 v196, v164, v165\n"
    "s_add_u32 s78, s78, s80\n"
    "v_mfma_f32_16x16x32_bf16 v[104:107], a[104:107], v[220:223], v[104:107]\n"
    "v_exp_f32 v166, v166\n"
    "v_mfma_f32_16x16x32_bf16 v[108:111], a[108:111], v[220:223], v[108:111]\n"
    "ds_read_b64_tr_b16 a[124:125], v235 offset:24576\n"
    "ds_read_b64_tr_b16 a[126:127], v235 offset:28672\n"
    "v_mfma_f32_16x16x32_bf16 v[176:179], a[80:83], a[0:3], v[144:147]\n"
    "v_exp_f32 v167, v167\n"
    "v_mfma_f32_16x16x32_bf16 v[176:179], a[84:87], a[4:7], v[176:179]\n"
    "v_cvt_pk_bf16_f32 v197, v166, v167\n"
    "s_sub_u32 s80, s80, 4\n"
    "v_mfma_f32_16x16x32_bf16 v[176:179], a[88:91], a[8:11], v[176:179]\n"
    "v_exp_f32 v168, v168\n"
    "v_mfma_f32_16x16x32_bf16 v[176:179], a[92:95], a[12:15], v[176:179]\n"
    "ds_read_b128 a[64:67], v224 offset:40960\n"
    "ds_read_b128 a[68:71], v225 offset:40960\n"
    "v_mfma_f32_16x16x32_bf16 v[180:183], a[80:83], a[16:19], v[148:151]\n"
    "v_exp_f32 v169, v169\n"
    "v_mfma_f32_16x16x32_bf16 v[180:183], a[84:87], a[20:23], v[180:183]\n"
    "v_cvt_pk_bf16_f32 v200, v168, v169\n"
    "s_max_i32 s80, s80, 70\n"
    "v_mfma_f32_16x16x32_bf16 v[180:183], a[88:91], a[24:27], v[180:183]\n"
    "v_exp_f32 v170, v170\n"
    "v_mfma_f32_16x16x32_bf16 v[180:183], a[92:95], a[28:31], v[180:183]\n"
    "ds_read_b128 a[72:75], v226 offset:40960\n"
    "ds_read_b128 a[76:79], v227 offset:40960\n"
    "v_mfma_f32_16x16x32_bf16 v[184:187], a[80:83], a[32:35], v[152:155]\n"
    "v_exp_f32 v171, v171\n"
    "v_mfma_f32_16x16x32_bf16 v[184:187], a[84:87], a[36:39], v[184:187]\n"
    "v_cvt_pk_bf16_f32 v201, v170, v171\n"
    "v_mfma_f32_16x16x32_bf16 v[184:187], a[88:91], a[40:43], v[184:187]\n"
    "v_exp_f32 v172, v172\n"
    "v_mfma_f32_16x16x32_bf16 v[184:187], a[92:95], a[44:47], v[184:187]\n"
    "buffer_load_dwordx4 v240, s[44:47], s56 offen offset:2048 nt lds\n"
    "v_mfma_f32_16x16x32_bf16 v[188:191], a[80:83], a[48:51], v[156:159]\n"
    "v_exp_f32 v173, v173\n"
    "v_mfma_f32_16x16x32_bf16 v[188:191], a[84:87], a[52:55], v[188:191]\n"
    "v_cvt_pk_bf16_f32 v204, v172, v173\n"
    "v_mfma_f32_16x16x32_bf16 v[188:191], a[88:91], a[56:59], v[188:191]\n"
    "v_exp_f32 v174, v174\n"
    "v_mfma_f32_16x16x32_bf16 v[188:191], a[92:95], a[60:63], v[188:191]\n"
    "s_waitcnt lgkmcnt(0)\n"
    "v_mfma_f32_16x16x32_bf16 v[16:19], a[112:115], v[208:211], v[16:19]\n"
    "v_exp_f32 v175, v175\n"
    "v_mfma_f32_16x16x32_bf16 v[20:23], a[116:119], v[208:211], v[20:23]\n"
    "buffer_load_dwordx4 v241, s[44:47], s56 offen offset:3072 nt lds\n"
    "v_cvt_pk_bf16_f32 v205, v174, v175\n"
    "s_add_u32 s56, s56, 16384\n"
    "v_mfma_f32_16x16x32_bf16 v[24:27], a[120:123], v[208:211], v[24:27]\n"
    "v_exp_f32 v176, v176\n"
    "v_mfma_f32_16x16x32_bf16 v[28:31], a[124:127], v[208:211], v[28:31]\n"
    "s_waitcnt vmcnt(12)\n"
    "s_barrier\n"
    "v_mfma_f32_16x16x32_bf16 v[48:51], a[112:115], v[212:215], v[48:51]\n"
    "v_exp_f32 v177, v177\n"
    "v_mfma_f32_16x16x32_bf16 v[52:55], a[116:119], v[212:215], v[52:55]\n"
    "v_cvt_pk_bf16_f32 v194, v176, v177\n"
    "v_mfma_f32_16x16x32_bf16 v[56:59], a[120:123], v[212:215], v[56:59]\n"
    "v_exp_f32 v178, v178\n"
    "v_mfma_f32_16x16x32_bf16 v[60:63], a[124:127], v[212:215], v[60:63]\n"
    "ds_read_b64_tr_b16 a[96:97], v228 offset:32768\n"
    "ds_read_b64_tr_b16 a[98:99], v228 offset:36864\n"
    "v_mfma_f32_16x16x32_bf16 v[80:83], a[112:115], v[216:219], v[80:83]\n"
    "v_exp_f32 v179, v179\n"
    "v_mfma_f32_16x16x32_bf16 v[84:87], a[116:119], v[216:219], v[84:87]\n"
    "v_cvt_pk_bf16_f32 v195, v178, v179\n"
    "v_mfma_f32_16x16x32_bf16 v[88:91], a[120:123], v[216:219], v[88:91]\n"
    "v_exp_f32 v180, v180\n"
    "v_mfma_f32_16x16x32_bf16 v[92:95], a[124:127], v[216:219], v[92:95]\n"
    "ds_read_b64_tr_b16 a[100:101], v229 offset:32768\n"
    "ds_read_b64_tr_b16 a[102:103], v229 offset:36864\n"
    "v_mfma_f32_16x16x32_bf16 v[112:115], a[112:115], v[220:223], v[112:115]\n"
    "v_exp_f32 v181, v181\n"
    "v_mfma_f32_16x16x32_bf16 v[116:119], a[116:119], v[220:223], v[116:119]\n"
    "v_cvt_pk_bf16_f32 v198, v180, v181\n"
    "v_mfma_f32_16x16x32_bf16 v[120:123], a[120:123], v[220:223], v[120:123]\n"
    "v_exp_f32 v182, v182\n"
    "v_mfma_f32_16x16x32_bf16 v[124:127], a[124:127], v[220:223], v[124:127]\n"
    "ds_read_b64_tr_b16 a[104:105], v230 offset:32768\n"
    "ds_read_b64_tr_b16 a[106:107], v230 offset:36864\n"
    "v_mfma_f32_16x16x32_bf16 v[128:131], a[128:131], v[208:211], v[128:131]\n"
    "v_exp_f32 v183, v183\n"
    "v_mfma_f32_16x16x32_bf16 v[132:135], a[128:131], v[212:215], v[132:135]\n"
    "v_cvt_pk_bf16_f32 v199, v182, v183\n"
    "v_mfma_f32_16x16x32_bf16 v[136:139], a[128:131], v[216:219], v[136:139]\n"
    "v_exp_f32 v184, v184\n"
    "v_mfma_f32_16x16x32_bf16 v[140:143], a[128:131], v[220:223], v[140:143]\n"
    "ds_read_b64_tr_b16 a[108:109], v231 offset:32768\n"
    "ds_read_b64_tr_b16 a[110:111], v231 offset:36864\n"
    "v_mfma_f32_16x16x32_bf16 v[160:163], a[64:67], a[0:3], v[144:147]\n"
    "v_exp_f32 v185, v185\n"
    "v_mfma_f32_16x16x32_bf16 v[160:163], a[68:71], a[4:7], v[160:163]\n"
    "v_cvt_pk_bf16_f32 v202, v184, v185\n"
    "v_mfma_f32_16x16x32_bf16 v[160:163], a[72:75], a[8:11], v[160:163]\n"
    "v_exp_f32 v186, v186\n"
    "v_mfma_f32_16x16x32_bf16 v[160:163], a[76:79], a[12:15], v[160:163]\n"
    "ds_read_b128 a[80:83], v224 offset:45056\n"
    "ds_read_b128 a[84:87], v225 offset:45056\n"
    "v_mfma_f32_16x16x32_bf16 v[164:167], a[64:67], a[16:19], v[148:151]\n"
    "v_exp_f32 v187, v187\n"
    "v_mfma_f32_16x16x32_bf16 v[164:167], a[68:71], a[20:23], v[164:167]\n"
    "v_cvt_pk_bf16_f32 v203, v186, v187\n"
    "v_mfma_f32_16x16x32_bf16 v[164:167], a[72:75], a[24:27], v[164:167]\n"
    "v_exp_f32 v188, v188\n"
    "v_mfma_f32_16x16x32_bf16 v[164:167], a[76:79], a[28:31], v[164:167]\n"
    "ds_read_b128 a[88:91], v226 offset:45056\n"
    "ds_read_b128 a[92:95], v227 offset:45056\n"
    "v_mfma_f32_16x16x32_bf16 v[168:171], a[64:67], a[32:35], v[152:155]\n"
    "v_exp_f32 v189, v189\n"
    "s_memrealtime s[76:77]\n"
    "v_mfma_f32_16x16x32_bf16 v[168:171], a[68:71], a[36:39], v[168:171]\n"
    "v_cvt_pk_bf16_f32 v206, v188, v189\n"
    "s_add_u32 m0, s57, 16384\n"
    "v_mfma_f32_16x16x32_bf16 v[168:171], a[72:75], a[40:43], v[168:171]\n"
    "v_exp_f32 v190, v190\n"
    "v_mfma_f32_16x16x32_bf16 v[168:171], a[76:79], a[44:47], v[168:171]\n"
    "buffer_load_dwordx4 v236, s[40:43], s56 offen nt lds\n"
    "v_mfma_f32_16x16x32_bf16 v[172:175], a[64:67], a[48:51], v[156:159]\n"
    "v_exp_f32 v191, v191\n"
    "v_mfma_f32_16x16x32_bf16 v[172:175], a[68:71], a[52:55], v[172:175]\n"
    "v_cvt_pk_bf16_f32 v207, v190, v191\n"
    "v_mfma_f32_16x16x32_bf16 v[172:175], a[72:75], a[56:59], v[172:175]\n"
    "s_waitcnt lgkmcnt(0)\n"
    "s_sub_u32 s79, s78, s76\n"
    "v_mfma_f32_16x16x32_bf16 v[172:175], a[76:79], a[60:63], v[172:175]\n"
    "s_cmp_gt_i32 s79, 0\n"
    "s_cbranch_scc1 Lpw23_%=\n"
    "Lpw23_%=b:\n"
    "v_mfma_f32_16x16x32_bf16 v[0:3], a[96:99], v[192:195], v[0:3]\n"
    "s_max_i32 s79, s79, -12\n"
    "s_add_u32 s78, s76, s79\n"
    "v_mfma_f32_16x16x32_bf16 v[4:7], a[100:103], v[192:195], v[4:7]\n"
    "buffer_load_dwordx4 v237, s[40:43], s56 offen offset:1024 nt lds\n"
    "v_mfma_f32_16x16x32_bf16 v[8:11], a[104:107], v[192:195], v[8:11]\n"
    "v_exp_f32 v160, v160\n"
    "v_mfma_f32_16x16x32_bf16 v[12:15], a[108:111], v[192:195], v[12:15]\n"
    "ds_read_b64_tr_b16 a[112:113], v232 offset:32768\n"
    "ds_read_b64_tr_b16 a[114:115], v232 offset:36864\n"
    "v_mfma_f32_16x16x32_bf16 v[32:35], a[96:99], v[196:199], v[32:35]\n"
    "v_exp_f32 v161, v161\n"
    "v_mfma_f32_16x16x32_bf16 v[36:39], a[100:103], v[196:199], v[36:39]\n"
    "v_cvt_pk_bf16_f32 v208, v160, v161\n"
    "v_mfma_f32_16x16x32_bf16 v[40:43], a[104:107], v[196:199], v[40:43]\n"
    "v_exp_f32 v162, v162\n"
    "v_mfma_f32_16x16x32_bf16 v[44:47], a[108:111], v[196:199], v[44:47]\n"
    "ds_read_b64_tr_b16 a[116:117], v233 offset:32768\n"
    "ds_read_b64_tr_b16 a[118:119], v233 offset:36864\n"
    "v_mfma_f32_16x16x32_bf16 v[64:67], a[96:99], v[200:203], v[64:67]\n"
    "v_exp_f32 v163, v163\n"
    "v_mfma_f32_16x16x32_bf16 v[68:71], a[100:103], v[200:203], v[68:71]\n"
    "v_cvt_pk_bf16_f32 v209, v162, v163\n"
    "v_mfma_f32_16x16x32_bf16 v[72:75], a[104:107], v[200:203], v[72:75]\n"
    "v_exp_f32 v164, v164\n"
    "v_mfma_f32_16x16x32_bf16 v[76:79], a[108:111], v[200:203], v[76:79]\n"
    "ds_read_b64_tr_b16 a[120:121], v234 offset:32768\n"
    "ds_read_b64_tr_b16 a[122:123], v234 offset:36864\n"
    "v_mfma_f32_16x16x32_bf16 v[96:99], a[96:99], v[204:207], v[96:99]\n"
    "v_exp_f32 v165, v165\n"
    "v_mfma_f32_16x16x32_bf16 v[100:103], a[100:103], v[204:207], v[100:103]\n"
    "v_cvt_pk_bf16_f32 v212, v164, v165\n"
    "s_add_u32 s78, s78, s80\n"
    "v_mfma_f32_16x16x32_bf16 v[104:107], a[104:107], v[204:207], v[104:107]\n"
    "v_exp_f32 v166, v166\n"
    "v_mfma_f32_16x16x32_bf16 v[108:111], a[108:111], v[204:207], v[108:111]\n"
    "ds_read_b64_tr_b16 a[124:125], v235 offset:32768\n"
    "ds_read_b64_tr_b16 a[126:127], v235 offset:36864\n"
    "v_mfma_f32_16x16x32_bf16 v[176:179], a[80:83], a[0:3], v[144:147]\n"
    "v_exp_f32 v167, v167\n"
    "v_mfma_f32_16x16x32_bf16 v[176:179], a[84:87], a[4:7], v[176:179]\n"
    "v_cvt_pk_bf16_f32 v213, v166, v167\n"
    "s_sub_u32 s80, s80, 4\n"
    "v_mfma_f32_16x16x32_bf16 v[176:179], a[88:91], a[8:11], v[176:179]\n"
    "v_exp_f32 v168, v168\n"
    "v_mfma_f32_16x16x32_bf16 v[176:179], a[92:95], a[12:15], v[176:179]\n"
    "ds_read_b128 a[64:67], v224 offset:49152\n"
    "ds_read_b128 a[68:71], v225 offset:49152\n"
    "v_mfma_f32_16x16x32_bf16 v[180:183], a[80:83], a[16:19], v[148:151]\n"
    "v_exp_f32 v169, v169\n"
    "v_mfma_f32_16x16x32_bf16 v[180:183], a[84:87], a[20:23], v[180:183]\n"
    "v_cvt_pk_bf16_f32 v216, v168, v169\n"
    "s_max_i32 s80, s80, 70\n"
    "v_mfma_f32_16x16x32_bf16 v[180:183], a[88:91], a[24:27], v[180:183]\n"
    "v_exp_f32 v170, v170\n"
    "v_mfma_f32_16x16x32_bf16 v[180:183], a[92:95], a[28:31], v[180:183]\n"
    "ds_read_b128 a[72:75], v226 offset:49152\n"
    "ds_read_b128 a[76:79], v227 offset:49152\n"
    "v_mfma_f32_16x16x32_bf16 v[184:187], a[80:83], a[32:35], v[152:155]\n"
    "v_exp_f32 v171, v171\n"
    "v_mfma_f32_16x16x32_bf16 v[184:187], a[84:87], a[36:39], v[184:187]\n"
    "v_cvt_pk_bf16_f32 v217, v170, v171\n"
    "v_mfma_f32_16x16x32_bf16 v[184:187], a[88:91], a[40:43], v[184:187]\n"
    "v_exp_f32 v172, v172\n"
    "v_mfma_f32_16x16x32_bf16 v[184:187], a[92:95], a[44:47], v[184:187]\n"
    "buffer_load_dwordx4 v238, s[40:43], s56 offen offset:2048 nt lds\n"
    "v_mfma_f32_16x16x32_bf16 v[188:191], a[80:83], a[48:51], v[156:159]\n"
    "v_exp_f32 v173, v173\n"
    "v_mfma_f32_16x16x32_bf16 v[188:191], a[84:87], a[52:55], v[188:191]\n"
    "v_cvt_pk_bf16_f32 v220, v172, v173\n"
    "v_mfma_f32_16x16x32_bf16 v[188:191], a[88:91], a[56:59], v[188:191]\n"
    "v_exp_f32 v174, v174\n"
    "v_mfma_f32_16x16x32_bf16 v[188:191], a[92:95], a[60:63], v[188:191]\n"
    "s_waitcnt lgkmcnt(0)\n"
    "v_mfma_f32_16x16x32_bf16 v[16:19], a[112:115], v[192:195], v[16:19]\n"
    "v_exp_f32 v175, v175\n"
    "v_mfma_f32_16x16x32_bf16 v[20:23], a[116:119], v[192:195], v[20:23]\n"
    "buffer_load_dwordx4 v239, s[40:43], s56 offen offset:3072 nt lds\n"
    "v_cvt_pk_bf16_f32 v221, v174, v175\n"
    "v_mfma_f32_16x16x32_bf16 v[24:27], a[120:123], v[192:195], v[24:27]\n"
    "v_exp_f32 v176, v176\n"
    "v_mfma_f32_16x16x32_bf16 v[28:31], a[124:127], v[192:195], v[28:31]\n"
    "v_mfma_f32_16x16x32_bf16 v[48:51], a[112:115], v[196:199], v[48:51]\n"
    "v_exp_f32 v177, v177\n"
    "v_mfma_f32_16x16x32_bf16 v[52:55], a[116:119], v[196:199], v[52:55]\n"
    "v_cvt_pk_bf16_f32 v210, v176, v177\n"
    "v_mfma_f32_16x16x32_bf16 v[56:59], a[120:123], v[196:199], v[56:59]\n"
    "v_exp_f32 v178, v178\n"
    "v_mfma_f32_16x16x32_bf16 v[60:63], a[124:127], v[196:199], v[60:63]\n"
    "ds_read_b64_tr_b16 a[96:97], v228 offset:40960\n"
    "ds_read_b64_tr_b16 a[98:99], v228 offset:45056\n"
    "v_mfma_f32_16x16x32_bf16 v[80:83], a[112:115], v[200:203], v[80:83]\n"
    "v_exp_f32 v179, v179\n"
    "v_mfma_f32_16x16x32_bf16 v[84:87], a[116:119], v[200:203], v[84:87]\n"
    "v_cvt_pk_bf16_f32 v211, v178, v179\n"
    "v_mfma_f32_16x16x32_bf16 v[88:91], a[120:123], v[200:203], v[88:91]\n"
    "v_exp_f32 v180, v180\n"
    "v_mfma_f32_16x16x32_bf16 v[92:95], a[124:127], v[200:203], v[92:95]\n"
    "ds_read_b64_tr_b16 a[100:101], v229 offset:40960\n"
    "ds_read_b64_tr_b16 a[102:103], v229 offset:45056\n"
    "v_mfma_f32_16x16x32_bf16 v[112:115], a[112:115], v[204:207], v[112:115]\n"
    "v_exp_f32 v181, v181\n"
    "v_mfma_f32_16x16x32_bf16 v[116:119], a[116:119], v[204:207], v[116:119]\n"
    "v_cvt_pk_bf16_f32 v214, v180, v181\n"
    "v_mfma_f32_16x16x32_bf16 v[120:123], a[120:123], v[204:207], v[120:123]\n"
    "v_exp_f32 v182, v182\n"
    "v_mfma_f32_16x16x32_bf16 v[124:127], a[124:127], v[204:207], v[124:127]\n"
    "ds_read_b64_tr_b16 a[104:105], v230 offset:40960\n"
    "ds_read_b64_tr_b16 a[106:107], v230 offset:45056\n"
    "v_mfma_f32_16x16x32_bf16 v[128:131], a[128:131], v[192:195], v[128:131]\n"
    "v_exp_f32 v183, v183\n"
    "v_mfma_f32_16x16x32_bf16 v[132:135], a[128:131], v[196:199], v[132:135]\n"
    "v_cvt_pk_bf16_f32 v215, v182, v183\n"
    "v_mfma_f32_16x16x32_bf16 v[136:139], a[128:131], v[200:203], v[136:139]\n"
    "v_exp_f32 v184, v184\n"
    "v_mfma_f32_16x16x32_bf16 v[140:143], a[128:131], v[204:207], v[140:143]\n"
    "ds_read_b64_tr_b16 a[108:109], v231 offset:40960\n"
    "ds_read_b64_tr_b16 a[110:111], v231 offset:45056\n"
    "v_mfma_f32_16x16x32_bf16 v[160:163], a[64:67], a[0:3], v[144:147]\n"
    "v_exp_f32 v185, v185\n"
    "v_mfma_f32_16x16x32_bf16 v[160:163], a[68:71], a[4:7], v[160:163]\n"
    "v_cvt_pk_bf16_f32 v218, v184, v185\n"
    "v_mfma_f32_16x16x32_bf16 v[160:163], a[72:75], a[8:11], v[160:163]\n"
    "v_exp_f32 v186, v186\n"
    "v_mfma_f32_16x16x32_bf16 v[160:163], a[76:79], a[12:15], v[160:163]\n"
    "ds_read_b128 a[80:83], v224 offset:53248\n"
    "ds_read_b128 a[84:87], v225 offset:53248\n"
    "v_mfma_f32_16x16x32_bf16 v[164:167], a[64:67], a[16:19], v[148:151]\n"
    "v_exp_f32 v187, v187\n"
    "v_mfma_f32_16x16x32_bf16 v[164:167], a[68:71], a[20:23], v[164:167]\n"
    "v_cvt_pk_bf16_f32 v219, v186, v187\n"
    "v_mfma_f32_16x16x32_bf16 v[164:167], a[72:75], a[24:27], v[164:167]\n"
    "v_exp_f32 v188, v188\n"
    "v_mfma_f32_16x16x32_bf16 v[164:167], a[76:79], a[28:31], v[164:167]\n"
    "ds_read_b128 a[88:91], v226 offset:53248\n"
    "ds_read_b128 a[92:95], v227 offset:53248\n"
    "v_mfma_f32_16x16x32_bf16 v[168:171], a[64:67], a[32:35], v[152:155]\n"
    "v_exp_f32 v189, v189\n"
    "s_memrealtime s[76:77]\n"
    "v_mfma_f32_16x16x32_bf16 v[168:171], a[68:71], a[36:39], v[168:171]\n"
    "v_cvt_pk_bf16_f32 v222, v188, v189\n"
    "s_add_u32 m0, s57, 81920\n"
    "v_mfma_f32_16x16x32_bf16 v[168:171], a[72:75], a[40:43], v[168:171]\n"
    "v_exp_f32 v190, v190\n"
    "v_mfma_f32_16x16x32_bf16 v[168:171], a[76:79], a[44:47], v[168:171]\n"
    "buffer_load_dwordx4 v240, s[44:47], s56 offen nt lds\n"
    "v_mfma_f32_16x16x32_bf16 v[172:175], a[64:67], a[48:51], v[156:159]\n"
    "v_exp_f32 v191, v191\n"
    "v_mfma_f32_16x16x32_bf16 v[172:175], a[68:71], a[52:55], v[172:175]\n"
    "v_cvt_pk_bf16_f32 v223, v190, v191\n"
    "v_mfma_f32_16x16x32_bf16 v[172:175], a[72:75], a[56:59], v[172:175]\n"
    "s_waitcnt lgkmcnt(0)\n"
    "s_sub_u32 s79, s78, s76\n"
    "v_mfma_f32_16x16x32_bf16 v[172:175], a[76:79], a[60:63], v[172:175]\n"
    "s_cmp_gt_i32 s79, 0\n"
    "s_cbranch_scc1 Lpw24_%=\n"
    "Lpw24_%=b:\n"
    "v_mfma_f32_16x16x32_bf16 v[0:3], a[96:99], v[208:211], v[0:3]\n"
    "s_max_i32 s79, s79, -12\n"
    "s_add_u32 s78, s76, s79\n"
    "v_mfma_f32_16x16x32_bf16 v[4:7], a[100:103], v[208:211], v[4:7]\n"
    "buffer_load_dwordx4 v241, s[44:47], s56 offen offset:1024 nt lds\n"
    "v_mfma_f32_16x16x32_bf16 v[8:11], a[104:107], v[208:211], v[8:11]\n"
    "v_exp_f32 v160, v160\n"
    "v_mfma_f32_16x16x32_bf16 v[12:15], a[108:111], v[208:211], v[12:15]\n"
    "ds_read_b64_tr_b16 a[112:113], v232 offset:40960\n"
    "ds_read_b64_tr_b16 a[114:115], v232 offset:45056\n"
    "v_mfma_f32_16x16x32_bf16 v[32:35], a[96:99], v[212:215], v[32:35]\n"
    "v_exp_f32 v161, v161\n"
    "v_mfma_f32_16x16x32_bf16 v[36:39], a[100:103], v[212:215], v[36:39]\n"
    "v_cvt_pk_bf16_f32 v192, v160, v161\n"
    "v_mfma_f32_16x16x32_bf16 v[40:43], a[104:107], v[212:215], v[40:43]\n"
    "v_exp_f32 v162, v162\n"
    "v_mfma_f32_16x16x32_bf16 v[44:47], a[108:111], v[212:215], v[44:47]\n"
    "ds_read_b64_tr_b16 a[116:117], v233 offset:40960\n"
    "ds_read_b64_tr_b16 a[118:119], v233 offset:45056\n"
    "v_mfma_f32_16x16x32_bf16 v[64:67], a[96:99], v[216:219], v[64:67]\n"
    "v_exp_f32 v163, v163\n"
    "v_mfma_f32_16x16x32_bf16 v[68:71], a[100:103], v[216:219], v[68:71]\n"
    "v_cvt_pk_bf16_f32 v193, v162, v163\n"
    "v_mfma_f32_16x16x32_bf16 v[72:75], a[104:107], v[216:219], v[72:75]\n"
    "v_exp_f32 v164, v164\n"
    "v_mfma_f32_16x16x32_bf16 v[76:79], a[108:111], v[216:219], v[76:79]\n"
    "ds_read_b64_tr_b16 a[120:121], v234 offset:40960\n"
    "ds_read_b64_tr_b16 a[122:123], v234 offset:45056\n"
    "v_mfma_f32_16x16x32_bf16 v[96:99], a[96:99], v[220:223], v[96:99]\n"
    "v_exp_f32 v165, v165\n"
    "v_mfma_f32_16x16x32_bf16 v[100:103], a[100:103], v[220:223], v[100:103]\n"
    "v_cvt_pk_bf16_f32 v196, v164, v165\n"
    "s_add_u32 s78, s78, s80\n"
    "v_mfma_f32_16x16x32_bf16 v[104:107], a[104:107], v[220:223], v[104:107]\n"
    "v_exp_f32 v166, v166\n"
    "v_mfma_f32_16x16x32_bf16 v[108:111], a[108:111], v[220:223], v[108:111]\n"
    "ds_read_b64_tr_b16 a[124:125], v235 offset:40960\n"
    "ds_read_b64_tr_b16 a[126:127], v235 offset:45056\n"
    "v_mfma_f32_16x16x32_bf16 v[176:179], a[80:83], a[0:3], v[144:147]\n"
    "v_exp_f32 v167, v167\n"
    "v_mfma_f32_16x16x32_bf16 v[176:179], a[84:87], a[4:7], v[176:179]\n"
    "v_cvt_pk_bf16_f32 v197, v166, v167\n"
    "s_sub_u32 s80, s80, 4\n"
    "v_mfma_f32_16x16x32_bf16 v[176:179], a[88:91], a[8:11], v[176:179]\n"
    "v_exp_f32 v168, v168\n"
    "v_mfma_f32_16x16x32_bf16 v[176:179], a[92:95], a[12:15], v[176:179]\n"
    "ds_read_b128 a[64:67], v224 offset:57344\n"
    "ds_read_b128 a[68:71], v225 offset:57344\n"
    "v_mfma_f32_16x16x32_bf16 v[180:183], a[80:83], a[16:19], v[148:151]\n"
    "v_exp_f32 v169, v169\n"
    "v_mfma_f32_16x16x32_bf16 v[180:183], a[84:87], a[20:23], v[180:183]\n"
    "v_cvt_pk_bf16_f32 v200, v168, v169\n"
    "s_max_i32 s80, s80, 70\n"
    "v_mfma_f32_16x16x32_bf16 v[180:183], a[88:91], a[24:27], v[180:183]\n"
    "v_exp_f32 v170, v170\n"
    "v_mfma_f32_16x16x32_bf16 v[180:183], a[92:95], a[28:31], v[180:183]\n"
    "ds_read_b128 a[72:75], v226 offset:57344\n"
    "ds_read_b128 a[76:79], v227 offset:57344\n"
    "v_mfma_f32_16x16x32_bf16 v[184:187], a[80:83], a[32:35], v[152:155]\n"
    "v_exp_f32 v171, v171\n"
    "v_mfma_f32_16x16x32_bf16 v[184:187], a[84:87], a[36:39], v[184:187]\n"
    "v_cvt_pk_bf16_f32 v201, v170, v171\n"
    "v_mfma_f32_16x16x32_bf16 v[184:187], a[88:91], a[40:43], v[184:187]\n"
    "v_exp_f32 v172, v172\n"
    "v_mfma_f32_16x16x32_bf16 v[184:187], a[92:95], a[44:47], v[184:187]\n"
    "buffer_load_dwordx4 v240, s[44:47], s56 offen offset:2048 nt lds\n"
    "v_mfma_f32_16x16x32_bf16 v[188:191], a[80:83], a[48:51], v[156:159]\n"
    "v_exp_f32 v173, v173\n"
    "v_mfma_f32_16x16x32_bf16 v[188:191], a[84:87], a[52:55], v[188:191]\n"
    "v_cvt_pk_bf16_f32 v204, v172, v173\n"
    "v_mfma_f32_16x16x32_bf16 v[188:191], a[88:91], a[56:59], v[188:191]\n"
    "v_exp_f32 v174, v174\n"
    "v_mfma_f32_16x16x32_bf16 v[188:191], a[92:95], a[60:63], v[188:191]\n"
    "s_waitcnt lgkmcnt(0)\n"
    "v_mfma_f32_16x16x32_bf16 v[16:19], a[112:115], v[208:211], v[16:19]\n"
    "v_exp_f32 v175, v175\n"
    "v_mfma_f32_16x16x32_bf16 v[20:23], a[116:119], v[208:211], v[20:23]\n"
    "buffer_load_dwordx4 v241, s[44:47], s56 offen offset:3072 nt lds\n"
    "v_cvt_pk_bf16_f32 v205, v174, v175\n"
    "s_add_u32 s56, s56, 16384\n"
    "v_mfma_f32_16x16x32_bf16 v[24:27], a[120:123], v[208:211], v[24:27]\n"
    "v_exp_f32 v176, v176\n"
    "v_mfma_f32_16x16x32_bf16 v[28:31], a[124:127], v[208:211], v[28:31]\n"
    "s_waitcnt vmcnt(12)\n"
    "s_barrier\n"
    "v_mfma_f32_16x16x32_bf16 v[48:51], a[112:115], v[212:215], v[48:51]\n"
    "v_exp_f32 v177, v177\n"
    "v_mfma_f32_16x16x32_bf16 v[52:55], a[116:119], v[212:215], v[52:55]\n"
    "v_cvt_pk_bf16_f32 v194, v176, v177\n"
    "v_mfma_f32_16x16x32_bf16 v[56:59], a[120:123], v[212:215], v[56:59]\n"
    "v_exp_f32 v178, v178\n"
    "v_mfma_f32_16x16x32_bf16 v[60:63], a[124:127], v[212:215], v[60:63]\n"
    "ds_read_b64_tr_b16 a[96:97], v228 offset:49152\n"
    "ds_read_b64_tr_b16 a[98:99], v228 offset:53248\n"
    "v_mfma_f32_16x16x32_bf16 v[80:83], a[112:115], v[216:219], v[80:83]\n"
    "v_exp_f32 v179, v179\n"
    "v_mfma_f32_16x16x32_bf16 v[84:87], a[116:119], v[216:219], v[84:87]\n"
    "v_cvt_pk_bf16_f32 v195, v178, v179\n"
    "v_mfma_f32_16x16x32_bf16 v[88:91], a[120:123], v[216:219], v[88:91]\n"
    "v_exp_f32 v180, v180\n"
    "v_mfma_f32_16x16x32_bf16 v[92:95], a[124:127], v[216:219], v[92:95]\n"
    "ds_read_b64_tr_b16 a[100:101], v229 offset:49152\n"
    "ds_read_b64_tr_b16 a[102:103], v229 offset:53248\n"
    "v_mfma_f32_16x16x32_bf16 v[112:115], a[112:115], v[220:223], v[112:115]\n"
    "v_exp_f32 v181, v181\n"
    "v_mfma_f32_16x16x32_bf16 v[116:119], a[116:119], v[220:223], v[116:119]\n"
    "v_cvt_pk_bf16_f32 v198, v180, v181\n"
    "v_mfma_f32_16x16x32_bf16 v[120:123], a[120:123], v[220:223], v[120:123]\n"
    "v_exp_f32 v182, v182\n"
    "v_mfma_f32_16x16x32_bf16 v[124:127], a[124:127], v[220:223], v[124:127]\n"
    "ds_read_b64_tr_b16 a[104:105], v230 offset:49152\n"
    "ds_read_b64_tr_b16 a[106:107], v230 offset:53248\n"
    "v_mfma_f32_16x16x32_bf16 v[128:131], a[128:131], v[208:211], v[128:131]\n"
    "v_exp_f32 v183, v183\n"
    "v_mfma_f32_16x16x32_bf16 v[132:135], a[128:131], v[212:215], v[132:135]\n"
    "v_cvt_pk_bf16_f32 v199, v182, v183\n"
    "v_mfma_f32_16x16x32_bf16 v[136:139], a[128:131], v[216:219], v[136:139]\n"
    "v_exp_f32 v184, v184\n"
    "v_mfma_f32_16x16x32_bf16 v[140:143], a[128:131], v[220:223], v[140:143]\n"
    "ds_read_b64_tr_b16 a[108:109], v231 offset:49152\n"
    "ds_read_b64_tr_b16 a[110:111], v231 offset:53248\n"
    "v_mfma_f32_16x16x32_bf16 v[160:163], a[64:67], a[0:3], v[144:147]\n"
    "v_exp_f32 v185, v185\n"
    "v_mfma_f32_16x16x32_bf16 v[160:163], a[68:71], a[4:7], v[160:163]\n"
    "v_cvt_pk_bf16_f32 v202, v184, v185\n"
    "v_mfma_f32_16x16x32_bf16 v[160:163], a[72:75], a[8:11], v[160:163]\n"
    "v_exp_f32 v186, v186\n"
    "v_mfma_f32_16x16x32_bf16 v[160:163], a[76:79], a[12:15], v[160:163]\n"
    "ds_read_b128 a[80:83], v224 offset:61440\n"
    "ds_read_b128 a[84:87], v225 offset:61440\n"
    "v_mfma_f32_16x16x32_bf16 v[164:167], a[64:67], a[16:19], v[148:151]\n"
    "v_exp_f32 v187, v187\n"
    "v_mfma_f32_16x16x32_bf16 v[164:167], a[68:71], a[20:23], v[164:167]\n"
    "v_cvt_pk_bf16_f32 v203, v186, v187\n"
    "v_mfma_f32_16x16x32_bf16 v[164:167], a[72:75], a[24:27], v[164:167]\n"
    "v_exp_f32 v188, v188\n"
    "v_mfma_f32_16x16x32_bf16 v[164:167], a[76:79], a[28:31], v[164:167]\n"
    "ds_read_b128 a[88:91], v226 offset:61440\n"
    "ds_read_b128 a[92:95], v227 offset:61440\n"
    "v_mfma_f32_16x16x32_bf16 v[168:171], a[64:67], a[32:35], v[152:155]\n"
    "v_exp_f32 v189, v189\n"
    "s_memrealtime s[76:77]\n"
    "v_mfma_f32_16x16x32_bf16 v[168:171], a[68:71], a[36:39], v[168:171]\n"
    "v_cvt_pk_bf16_f32 v206, v188, v189\n"
    "s_add_u32 m0, s57, 32768\n"
    "v_mfma_f32_16x16x32_bf16 v[168:171], a[72:75], a[40:43], v[168:171]\n"
    "v_exp_f32 v190, v190\n"
    "v_mfma_f32_16x16x32_bf16 v[168:171], a[76:79], a[44:47], v[168:171]\n"
    "buffer_load_dwordx4 v236, s[40:43], s56 offen nt lds\n"
    "v_mfma_f32_16x16x32_bf16 v[172:175], a[64:67], a[48:51], v[156:159]\n"
    "v_exp_f32 v191, v191\n"
    "v_mfma_f32_16x16x32_bf16 v[172:175], a[68:71], a[52:55], v[172:175]\n"
    "v_cvt_pk_bf16_f32 v207, v190, v191\n"
    "v_mfma_f32_16x16x32_bf16 v[172:175], a[72:75], a[56:59], v[172:175]\n"
    "s_waitcnt lgkmcnt(0)\n"
    "s_sub_u32 s79, s78, s76\n"
    "v_mfma_f32_16x16x32_bf16 v[172:175], a[76:79], a[60:63], v[172:175]\n"
    "s_cmp_gt_i32 s79, 0\n"
    "s_cbranch_scc1 Lpw25_%=\n"
    "Lpw25_%=b:\n"
    "v_mfma_f32_16x16x32_bf16 v[0:3], a[96:99], v[192:195], v[0:3]\n"
    "s_max_i32 s79, s79, -12\n"
    "s_add_u32 s78, s76, s79\n"
    "v_mfma_f32_16x16x32_bf16 v[4:7], a[100:103], v[192:195], v[4:7]\n"
    "buffer_load_dwordx4 v237, s[40:43], s56 offen offset:1024 nt lds\n"
    "v_mfma_f32_16x16x32_bf16 v[8:11], a[104:107], v[192:195], v[8:11]\n"
    "v_exp_f32 v160, v160\n"
    "v_mfma_f32_16x16x32_bf16 v[12:15], a[108:111], v[192:195], v[12:15]\n"
    "ds_read_b64_tr_b16 a[112:113], v232 offset:49152\n"
    "ds_read_b64_tr_b16 a[114:115], v232 offset:53248\n"
    "v_mfma_f32_16x16x32_bf16 v[32:35], a[96:99], v[196:199], v[32:35]\n"
    "v_exp_f32 v161, v161\n"
    "v_mfma_f32_16x16x32_bf16 v[36:39], a[100:103], v[196:199], v[36:39]\n"
    "v_cvt_pk_bf16_f32 v208, v160, v161\n"
    "v_mfma_f32_16x16x32_bf16 v[40:43], a[104:107], v[196:199], v[40:43]\n"
    "v_exp_f32 v162, v162\n"
    "v_mfma_f32_16x16x32_bf16 v[44:47], a[108:111], v[196:199], v[44:47]\n"
    "ds_read_b64_tr_b16 a[116:117], v233 offset:49152\n"
    "ds_read_b64_tr_b16 a[118:119], v233 offset:53248\n"
    "v_mfma_f32_16x16x32_bf16 v[64:67], a[96:99], v[200:203], v[64:67]\n"
    "v_exp_f32 v163, v163\n"
    "v_mfma_f32_16x16x32_bf16 v[68:71], a[100:103], v[200:203], v[68:71]\n"
    "v_cvt_pk_bf16_f32 v209, v162, v163\n"
    "v_mfma_f32_16x16x32_bf16 v[72:75], a[104:107], v[200:203], v[72:75]\n"
    "v_exp_f32 v164, v164\n"
    "v_mfma_f32_16x16x32_bf16 v[76:79], a[108:111], v[200:203], v[76:79]\n"
    "ds_read_b64_tr_b16 a[120:121], v234 offset:49152\n"
    "ds_read_b64_tr_b16 a[122:123], v234 offset:53248\n"
    "v_mfma_f32_16x16x32_bf16 v[96:99], a[96:99], v[204:207], v[96:99]\n"
    "v_exp_f32 v165, v165\n"
    "v_mfma_f32_16x16x32_bf16 v[100:103], a[100:103], v[204:207], v[100:103]\n"
    "v_cvt_pk_bf16_f32 v212, v164, v165\n"
    "s_add_u32 s78, s78, s80\n"
    "v_mfma_f32_16x16x32_bf16 v[104:107], a[104:107], v[204:207], v[104:107]\n"
    "v_exp_f32 v166, v166\n"
    "v_mfma_f32_16x16x32_bf16 v[108:111], a[108:111], v[204:207], v[108:111]\n"
    "ds_read_b64_tr_b16 a[124:125], v235 offset:49152\n"
    "ds_read_b64_tr_b16 a[126:127], v235 offset:53248\n"
    "v_mfma_f32_16x16x32_bf16 v[176:179], a[80:83], a[0:3], v[144:147]\n"
    "v_exp_f32 v167, v167\n"
    "v_mfma_f32_16x16x32_bf16 v[176:179], a[84:87], a[4:7], v[176:179]\n"
    "v_cvt_pk_bf16_f32 v213, v166, v167\n"
    "s_sub_u32 s80, s80, 4\n"
    "v_mfma_f32_16x16x32_bf16 v[176:179], a[88:91], a[8:11], v[176:179]\n"
    "v_exp_f32 v168, v168\n"
    "v_mfma_f32_16x16x32_bf16 v[176:179], a[92:95], a[12:15], v[176:179]\n"
    "ds_read_b128 a[64:67], v224\n"
    "ds_read_b128 a[68:71], v225\n"
    "v_mfma_f32_16x16x32_bf16 v[180:183], a[80:83], a[16:19], v[148:151]\n"
    "v_exp_f32 v169, v169\n"
    "v_mfma_f32_16x16x32_bf16 v[180:183], a[84:87], a[20:23], v[180:183]\n"
    "v_cvt_pk_bf16_f32 v216, v168, v169\n"
    "s_max_i32 s80, s80, 70\n"
    "v_mfma_f32_16x16x32_bf16 v[180:183], a[88:91], a[24:27], v[180:183]\n"
    "v_exp_f32 v170, v170\n"
    "v_mfma_f32_16x16x32_bf16 v[180:183], a[92:95], a[28:31], v[180:183]\n"
    "ds_read_b128 a[72:75], v226\n"
    "ds_read_b128 a[76:79], v227\n"
    "v_mfma_f32_16x16x32_bf16 v[184:187], a[80:83], a[32:35], v[152:155]\n"
    "v_exp_f32 v171, v171\n"
    "v_mfma_f32_16x16x32_bf16 v[184:187], a[84:87], a[36:39], v[184:187]\n"
    "v_cvt_pk_bf16_f32 v217, v170, v171\n"
    "v_mfma_f32_16x16x32_bf16 v[184:187], a[88:91], a[40:43], v[184:187]\n"
    "v_exp_f32 v172, v172\n"
    "v_mfma_f32_16x16x32_bf16 v[184:187], a[92:95], a[44:47], v[184:187]\n"
    "buffer_load_dwordx4 v238, s[40:43], s56 offen offset:2048 nt lds\n"
    "v_mfma_f32_16x16x32_bf16 v[188:191], a[80:83], a[48:51], v[156:159]\n"
    "v_exp_f32 v173, v173\n"
    "v_mfma_f32_16x16x32_bf16 v[188:191], a[84:87], a[52:55], v[188:191]\n"
    "v_cvt_pk_bf16_f32 v220, v172, v173\n"
    "v_mfma_f32_16x16x32_bf16 v[188:191], a[88:91], a[56:59], v[188:191]\n"
    "v_exp_f32 v174, v174\n"
    "v_mfma_f32_16x16x32_bf16 v[188:191], a[92:95], a[60:63], v[188:191]\n"
    "s_waitcnt lgkmcnt(0)\n"
    "v_mfma_f32_16x16x32_bf16 v[16:19], a[112:115], v[192:195], v[16:19]\n"
    "v_exp_f32 v175, v175\n"
    "v_mfma_f32_16x16x32_bf16 v[20:23], a[116:119], v[192:195], v[20:23]\n"
    "buffer_load_dwordx4 v239, s[40:43], s56 offen offset:3072 nt lds\n"
    "v_cvt_pk_bf16_f32 v221, v174, v175\n"
    "v_mfma_f32_16x16x32_bf16 v[24:27], a[120:123], v[192:195], v[24:27]\n"
    "v_exp_f32 v176, v176\n"
    "v_mfma_f32_16x16x32_bf16 v[28:31], a[124:127], v[192:195], v[28:31]\n"
    "v_mfma_f32_16x16x32_bf16 v[48:51], a[112:115], v[196:199], v[48:51]\n"
    "v_exp_f32 v177, v177\n"
    "v_mfma_f32_16x16x32_bf16 v[52:55], a[116:119], v[196:199], v[52:55]\n"
    "v_cvt_pk_bf16_f32 v210, v176, v177\n"
    "v_mfma_f32_16x16x32_bf16 v[56:59], a[120:123], v[196:199], v[56:59]\n"
    "v_exp_f32 v178, v178\n"
    "v_mfma_f32_16x16x32_bf16 v[60:63], a[124:127], v[196:199], v[60:63]\n"
    "ds_read_b64_tr_b16 a[96:97], v228 offset:57344\n"
    "ds_read_b64_tr_b16 a[98:99], v228 offset:61440\n"
    "v_mfma_f32_16x16x32_bf16 v[80:83], a[112:115], v[200:203], v[80:83]\n"
    "v_exp_f32 v179, v179\n"
    "v_mfma_f32_16x16x32_bf16 v[84:87], a[116:119], v[200:203], v[84:87]\n"
    "v_cvt_pk_bf16_f32 v211, v178, v179\n"
    "v_mfma_f32_16x16x32_bf16 v[88:91], a[120:123], v[200:203], v[88:91]\n"
    "v_exp_f32 v180, v180\n"
    "v_mfma_f32_16x16x32_bf16 v[92:95], a[124:127], v[200:203], v[92:95]\n"
    "ds_read_b64_tr_b16 a[100:101], v229 offset:57344\n"
    "ds_read_b64_tr_b16 a[102:103], v229 offset:61440\n"
    "v_mfma_f32_16x16x32_bf16 v[112:115], a[112:115], v[204:207], v[112:115]\n"
    "v_exp_f32 v181, v181\n"
    "v_mfma_f32_16x16x32_bf16 v[116:119], a[116:119], v[204:207], v[116:119]\n"
    "v_cvt_pk_bf16_f32 v214, v180, v181\n"
    "v_mfma_f32_16x16x32_bf16 v[120:123], a[120:123], v[204:207], v[120:123]\n"
    "v_exp_f32 v182, v182\n"
    "v_mfma_f32_16x16x32_bf16 v[124:127], a[124:127], v[204:207], v[124:127]\n"
    "ds_read_b64_tr_b16 a[104:105], v230 offset:57344\n"
    "ds_read_b64_tr_b16 a[106:107], v230 offset:61440\n"
    "v_mfma_f32_16x16x32_bf16 v[128:131], a[128:131], v[192:195], v[128:131]\n"
    "v_exp_f32 v183, v183\n"
    "v_mfma_f32_16x16x32_bf16 v[132:135], a[128:131], v[196:199], v[132:135]\n"
    "v_cvt_pk_bf16_f32 v215, v182, v183\n"
    "v_mfma_f32_16x16x32_bf16 v[136:139], a[128:131], v[200:203], v[136:139]\n"
    "v_exp_f32 v184, v184\n"
    "v_mfma_f32_16x16x32_bf16 v[140:143], a[128:131], v[204:207], v[140:143]\n"
    "ds_read_b64_tr_b16 a[108:109], v231 offset:57344\n"
    "ds_read_b64_tr_b16 a[110:111], v231 offset:61440\n"
    "s_cmp_eq_u32 s58, 1\n"
    "s_cbranch_scc1 Ltail_%=\n"
    "v_mfma_f32_16x16x32_bf16 v[160:163], a[64:67], a[0:3], v[144:147]\n"
    "v_exp_f32 v185, v185\n"
    "v_mfma_f32_16x16x32_bf16 v[160:163], a[68:71], a[4:7], v[160:163]\n"
    "v_cvt_pk_bf16_f32 v218, v184, v185\n"
    "v_mfma_f32_16x16x32_bf16 v[160:163], a[72:75], a[8:11], v[160:163]\n"
    "v_exp_f32 v186, v186\n"
    "v_mfma_f32_16x16x32_bf16 v[160:163], a[76:79], a[12:15], v[160:163]\n"
    "ds_read_b128 a[80:83], v224 offset:4096\n"
    "ds_read_b128 a[84:87], v225 offset:4096\n"
    "v_mfma_f32_16x16x32_bf16 v[164:167], a[64:67], a[16:19], v[148:151]\n"
    "v_exp_f32 v187, v187\n"
    "v_mfma_f32_16x16x32_bf16 v[164:167], a[68:71], a[20:23], v[164:167]\n"
    "v_cvt_pk_bf16_f32 v219, v186, v187\n"
    "v_mfma_f32_16x16x32_bf16 v[164:167], a[72:75], a[24:27], v[164:167]\n"
    "v_exp_f32 v188, v188\n"
    "v_mfma_f32_16x16x32_bf16 v[164:167], a[76:79], a[28:31], v[164:167]\n"
    "ds_read_b128 a[88:91], v226 offset:4096\n"
    "ds_read_b128 a[92:95], v227 offset:4096\n"
    "v_mfma_f32_16x16x32_bf16 v[168:171], a[64:67], a[32:35], v[152:155]\n"
    "v_exp_f32 v189, v189\n"
    "s_memrealtime s[76:77]\n"
    "v_mfma_f32_16x16x32_bf16 v[168:171], a[68:71], a[36:39], v[168:171]\n"
    "v_cvt_pk_bf16_f32 v222, v188, v189\n"
    "s_add_u32 m0, s57, 98304\n"
    "v_mfma_f32_16x16x32_bf16 v[168:171], a[72:75], a[40:43], v[168:171]\n"
    "v_exp_f32 v190, v190\n"
    "v_mfma_f32_16x16x32_bf16 v[168:171], a[76:79], a[44:47], v[168:171]\n"
    "buffer_load_dwordx4 v240, s[44:47], s56 offen nt lds\n"
    "v_mfma_f32_16x16x32_bf16 v[172:175], a[64:67], a[48:51], v[156:159]\n"
    "v_exp_f32 v191, v191\n"
    "v_mfma_f32_16x16x32_bf16 v[172:175], a[68:71], a[52:55], v[172:175]\n"
    "v_cvt_pk_bf16_f32 v223, v190, v191\n"
    "v_mfma_f32_16x16x32_bf16 v[172:175], a[72:75], a[56:59], v[172:175]\n"
    "s_waitcnt lgkmcnt(0)\n"
    "s_sub_u32 s79, s78, s76\n"
    "v_mfma_f32_16x16x32_bf16 v[172:175], a[76:79], a[60:63], v[172:175]\n"
    "s_cmp_gt_i32 s79, 0\n"
    "s_cbranch_scc1 Lpw26_%=\n"
    "Lpw26_%=b:\n"
    "v_mfma_f32_16x16x32_bf16 v[0:3], a[96:99], v[208:211], v[0:3]\n"
    "s_max_i32 s79, s79, -12\n"
    "s_add_u32 s78, s76, s79\n"
    "v_mfma_f32_16x16x32_bf16 v[4:7], a[100:103], v[208:211], v[4:7]\n"
    "buffer_load_dwordx4 v241, s[44:47], s56 offen offset:1024 nt lds\n"
    "v_mfma_f32_16x16x32_bf16 v[8:11], a[104:107], v[208:211], v[8:11]\n"
    "v_exp_f32 v160, v160\n"
    "v_mfma_f32_16x16x32_bf16 v[12:15], a[108:111], v[208:211], v[12:15]\n"
    "ds_read_b64_tr_b16 a[112:113], v232 offset:57344\n"
    "ds_read_b64_tr_b16 a[114:115], v232 offset:61440\n"
    "v_mfma_f32_16x16x32_bf16 v[32:35], a[96:99], v[212:215], v[32:35]\n"
    "v_exp_f32 v161, v161\n"
    "v_mfma_f32_16x16x32_bf16 v[36:39], a[100:103], v[212:215], v[36:39]\n"
    "v_cvt_pk_bf16_f32 v192, v160, v161\n"
    "v_mfma_f32_16x16x32_bf16 v[40:43], a[104:107], v[212:215], v[40:43]\n"
    "v_exp_f32 v162, v162\n"
    "v_mfma_f32_16x16x32_bf16 v[44:47], a[108:111], v[212:215], v[44:47]\n"
    "ds_read_b64_tr_b16 a[116:117], v233 offset:57344\n"
    "ds_read_b64_tr_b16 a[118:119], v233 offset:61440\n"
    "v_mfma_f32_16x16x32_bf16 v[64:67], a[96:99], v[216:219], v[64:67]\n"
    "v_exp_f32 v163, v163\n"
    "v_mfma_f32_16x16x32_bf16 v[68:71], a[100:103], v[216:219], v[68:71]\n"
    "v_cvt_pk_bf16_f32 v193, v162, v163\n"
    "v_mfma_f32_16x16x32_bf16 v[72:75], a[104:107], v[216:219], v[72:75]\n"
    "v_exp_f32 v164, v164\n"
    "v_mfma_f32_16x16x32_bf16 v[76:79], a[108:111], v[216:219], v[76:79]\n"
    "ds_read_b64_tr_b16 a[120:121], v234 offset:57344\n"
    "ds_read_b64_tr_b16 a[122:123], v234 offset:61440\n"
    "v_mfma_f32_16x16x32_bf16 v[96:99], a[96:99], v[220:223], v[96:99]\n"
    "v_exp_f32 v165, v165\n"
    "v_mfma_f32_16x16x32_bf16 v[100:103], a[100:103], v[220:223], v[100:103]\n"
    "v_cvt_pk_bf16_f32 v196, v164, v165\n"
    "s_add_u32 s78, s78, s80\n"
    "v_mfma_f32_16x16x32_bf16 v[104:107], a[104:107], v[220:223], v[104:107]\n"
    "v_exp_f32 v166, v166\n"
    "v_mfma_f32_16x16x32_bf16 v[108:111], a[108:111], v[220:223], v[108:111]\n"
    "ds_read_b64_tr_b16 a[124:125], v235 offset:57344\n"
    "ds_read_b64_tr_b16 a[126:127], v235 offset:61440\n"
    "v_mfma_f32_16x16x32_bf16 v[176:179], a[80:83], a[0:3], v[144:147]\n"
    "v_exp_f32 v167, v167\n"
    "v_mfma_f32_16x16x32_bf16 v[176:179], a[84:87], a[4:7], v[176:179]\n"
    "v_cvt_pk_bf16_f32 v197, v166, v167\n"
    "s_sub_u32 s80, s80, 4\n"
    "v_mfma_f32_16x16x32_bf16 v[176:179], a[88:91], a[8:11], v[176:179]\n"
    "v_exp_f32 v168, v168\n"
    "v_mfma_f32_16x16x32_bf16 v[176:179], a[92:95], a[12:15], v[176:179]\n"
    "ds_read_b128 a[64:67], v224 offset:8192\n"
    "ds_read_b128 a[68:71], v225 offset:8192\n"
    "v_mfma_f32_16x16x32_bf16 v[180:183], a[80:83], a[16:19], v[148:151]\n"
    "v_exp_f32 v169, v169\n"
    "v_mfma_f32_16x16x32_bf16 v[180:183], a[84:87], a[20:23], v[180:183]\n"
    "v_cvt_pk_bf16_f32 v200, v168, v169\n"
    "s_max_i32 s80, s80, 70\n"
    "s_memtime s[82:83]\n"
    "v_mfma_f32_16x16x32_bf16 v[180:183], a[88:91], a[24:27], v[180:183]\n"
    "v_exp_f32 v170, v170\n"
    "v_mfma_f32_16x16x32_bf16 v[180:183], a[92:95], a[28:31], v[180:183]\n"
    "ds_read_b128 a[72:75], v226 offset:8192\n"
    "ds_read_b128 a[76:79], v227 offset:8192\n"
    "s_memrealtime s[84:85]\n"
    "v_mfma_f32_16x16x32_bf16 v[184:187], a[80:83], a[32:35], v[152:155]\n"
    "v_exp_f32 v171, v171\n"
    "v_mfma_f32_16x16x32_bf16 v[184:187], a[84:87], a[36:39], v[184:187]\n"
    "v_cvt_pk_bf16_f32 v201, v170, v171\n"
    "v_mfma_f32_16x16x32_bf16 v[184:187], a[88:91], a[40:43], v[184:187]\n"
    "v_exp_f32 v172, v172\n"
    "v_mfma_f32_16x16x32_bf16 v[184:187], a[92:95], a[44:47], v[184:187]\n"
    "buffer_load_dwordx4 v240, s[44:47], s56 offen offset:2048 nt lds\n"
    "v_mfma_f32_16x16x32_bf16 v[188:191], a[80:83], a[48:51], v[156:159]\n"
    "v_exp_f32 v173, v173\n"
    "v_mfma_f32_16x16x32_bf16 v[188:191], a[84:87], a[52:55], v[188:191]\n"
    "v_cvt_pk_bf16_f32 v204, v172, v173\n"
    "v_mfma_f32_16x16x32_bf16 v[188:191], a[88:91], a[56:59], v[188:191]\n"
    "v_exp_f32 v174, v174\n"
    "v_mfma_f32_16x16x32_bf16 v[188:191], a[92:95], a[60:63], v[188:191]\n"
    "s_waitcnt lgkmcnt(0)\n"
    "v_mfma_f32_16x16x32_bf16 v[16:19], a[112:115], v[208:211], v[16:19]\n"
    "v_exp_f32 v175, v175\n"
    "v_mfma_f32_16x16x32_bf16 v[20:23], a[116:119], v[208:211], v[20:23]\n"
    "buffer_load_dwordx4 v241, s[44:47], s56 offen offset:3072 nt lds\n"
    "v_cvt_pk_bf16_f32 v205, v174, v175\n"
    "s_add_u32 s56, s56, 16384\n"
    "s_sub_u32 s86, s82, s86\n"
    "v_mfma_f32_16x16x32_bf16 v[24:27], a[120:123], v[208:211], v[24:27]\n"
    "v_exp_f32 v176, v176\n"
    "v_mfma_f32_16x16x32_bf16 v[28:31], a[124:127], v[208:211], v[28:31]\n"
    "s_waitcnt vmcnt(12)\n"
    "s_barrier\n"
    "v_mfma_f32_16x16x32_bf16 v[48:51], a[112:115], v[212:215], v[48:51]\n"
    "v_exp_f32 v177, v177\n"
    "v_mfma_f32_16x16x32_bf16 v[52:55], a[116:119], v[212:215], v[52:55]\n"
    "v_cvt_pk_bf16_f32 v194, v176, v177\n"
    "s_sub_u32 s87, s84, s87\n"
    "v_mfma_f32_16x16x32_bf16 v[56:59], a[120:123], v[212:215], v[56:59]\n"
    "v_exp_f32 v178, v178\n"
    "v_mfma_f32_16x16x32_bf16 v[60:63], a[124:127], v[212:215], v[60:63]\n"
    "ds_read_b64_tr_b16 a[96:97], v228\n"
    "ds_read_b64_tr_b16 a[98:99], v228 offset:4096\n"
    "v_mfma_f32_16x16x32_bf16 v[80:83], a[112:115], v[216:219], v[80:83]\n"
    "v_exp_f32 v179, v179\n"
    "v_mfma_f32_16x16x32_bf16 v[84:87], a[116:119], v[216:219], v[84:87]\n"
    "v_cvt_pk_bf16_f32 v195, v178, v179\n"
    "s_lshl_b32 s86, s86, 1\n"
    "s_mul_i32 s87, s87, 32\n"
    "v_mfma_f32_16x16x32_bf16 v[88:91], a[120:123], v[216:219], v[88:91]\n"
    "v_exp_f32 v180, v180\n"
    "v_mfma_f32_16x16x32_bf16 v[92:95], a[124:127], v[216:219], v[92:95]\n"
    "ds_read_b64_tr_b16 a[100:101], v229\n"
    "ds_read_b64_tr_b16 a[102:103], v229 offset:4096\n"
    "v_mfma_f32_16x16x32_bf16 v[112:115], a[112:115], v[220:223], v[112:115]\n"
    "v_exp_f32 v181, v181\n"
    "v_mfma_f32_16x16x32_bf16 v[116:119], a[116:119], v[220:223], v[116:119]\n"
    "v_cvt_pk_bf16_f32 v198, v180, v181\n"
    "s_cmp_gt_u32 s86, s87\n"
    "s_cselect_b32 s88, 1, 0\n"
    "v_mfma_f32_16x16x32_bf16 v[120:123], a[120:123], v[220:223], v[120:123]\n"
    "v_exp_f32 v182, v182\n"
    "v_mfma_f32_16x16x32_bf16 v[124:127], a[124:127], v[220:223], v[124:127]\n"
    "ds_read_b64_tr_b16 a[104:105], v230\n"
    "ds_read_b64_tr_b16 a[106:107], v230 offset:4096\n"
    "v_mfma_f32_16x16x32_bf16 v[128:131], a[128:131], v[208:211], v[128:131]\n"
    "v_exp_f32 v183, v183\n"
    "v_mfma_f32_16x16x32_bf16 v[132:135], a[128:131], v[212:215], v[132:135]\n"
    "v_cvt_pk_bf16_f32 v199, v182, v183\n"
    "s_mov_b32 s86, s82\n"
    "s_mov_b32 s87, s84\n"
    "v_mfma_f32_16x16x32_bf16 v[136:139], a[128:131], v[216:219], v[136:139]\n"
    "v_exp_f32 v184, v184\n"
    "v_mfma_f32_16x16x32_bf16 v[140:143], a[128:131], v[220:223], v[140:143]\n"
    "ds_read_b64_tr_b16 a[108:109], v231\n"
    "ds_read_b64_tr_b16 a[110:111], v231 offset:4096\n"
    "s_sub_u32 s58, s58, 1\n"
    "s_cmp_eq_u32 s88, 0\n"
    "s_cbranch_scc1 Lfast_%=\n"
    "s_branch Lslow_%=\n"
    "Ltail_%=:\n"
    "ds_read_b64_tr_b16 a[112:113], v232 offset:57344\n"
    "ds_read_b64_tr_b16 a[114:115], v232 offset:61440\n"
    "ds_read_b64_tr_b16 a[116:117], v233 offset:57344\n"
    "ds_read_b64_tr_b16 a[118:119], v233 offset:61440\n"
    "ds_read_b64_tr_b16 a[120:121], v234 offset:57344\n"
    "ds_read_b64_tr_b16 a[122:123], v234 offset:61440\n"
    "ds_read_b64_tr_b16 a[124:125], v235 offset:57344\n"
    "ds_read_b64_tr_b16 a[126:127], v235 offset:61440\n"
    "s_mov_b64 s[66:67], -1\n"
    "s_waitcnt lgkmcnt(8)\n"
    "v_mfma_f32_16x16x32_bf16 v[0:3], a[96:99], v[208:211], v[0:3]\n"
    "v_exp_f32 v185, v185\n"
    "v_exp_f32 v186, v186\n"
    "v_mfma_f32_16x16x32_bf16 v[4:7], a[100:103], v[208:211], v[4:7]\n"
    "v_cvt_pk_bf16_f32 v218, v184, v185\n"
    "v_exp_f32 v187, v187\n"
    "v_mfma_f32_16x16x32_bf16 v[8:11], a[104:107], v[208:211], v[8:11]\n"
    "v_exp_f32 v188, v188\n"
    "v_cvt_pk_bf16_f32 v219, v186, v187\n"
    "v_mfma_f32_16x16x32_bf16 v[12:15], a[108:111], v[208:211], v[12:15]\n"
    "v_exp_f32 v189, v189\n"
    "v_exp_f32 v190, v190\n"
    "s_waitcnt lgkmcnt(0)\n"
    "v_mfma_f32_16x16x32_bf16 v[16:19], a[112:115], v[208:211], v[16:19]\n"
    "v_cvt_pk_bf16_f32 v222, v188, v189\n"
    "v_exp_f32 v191, v191\n"
    "v_mfma_f32_16x16x32_bf16 v[20:23], a[116:119], v[208:211], v[20:23]\n"
    "s_nop 0\n"
    "v_cvt_pk_bf16_f32 v223, v190, v191\n"
    "v_mfma_f32_16x16x32_bf16 v[24:27], a[120:123], v[208:211], v[24:27]\n"
    "v_mfma_f32_16x16x32_bf16 v[28:31], a[124:127], v[208:211], v[28:31]\n"
    "v_mfma_f32_16x16x32_bf16 v[128:131], a[128:131], v[208:211], v[128:131]\n"
    "v_mfma_f32_16x16x32_bf16 v[32:35], a[96:99], v[212:215], v[32:35]\n"
    "v_mfma_f32_16x16x32_bf16 v[36:39], a[100:103], v[212:215], v[36:39]\n"
    "v_mfma_f32_16x16x32_bf16 v[40:43], a[104:107], v[212:215], v[40:43]\n"
    "v_mfma_f32_16x16x32_bf16 v[44:47], a[108:111], v[212:215], v[44:47]\n"
    "s_nop 3\n"
    "v_cmp_ge_f32 vcc, s62, v128\n"
    "v_rcp_f32 v130, v128\n"
    "v_mfma_f32_16x16x32_bf16 v[48:51], a[112:115], v[212:215], v[48:51]\n"
    "s_nop 1\n"
    "s_and_b64 s[66:67], s[66:67], vcc\n"
    "v_pk_mul_f32 v[0:1], v[0:1], v[130:131] op_sel_hi:[1,0]\n"
    "v_mfma_f32_16x16x32_bf16 v[52:55], a[116:119], v[212:215], v[52:55]\n"
    "v_pk_mul_f32 v[2:3], v[2:3], v[130:131] op_sel_hi:[1,0]\n"
    "v_cvt_pk_bf16_f32 v0, v0, v1\n"
    "v_cvt_pk_bf16_f32 v1, v2, v3\n"
    "v_mfma_f32_16x16x32_bf16 v[56:59], a[120:123], v[212:215], v[56:59]\n"
    "ds_write_b64 v244, v[0:1]\n"
    "v_pk_mul_f32 v[4:5], v[4:5], v[130:131] op_sel_hi:[1,0]\n"
    "v_pk_mul_f32 v[6:7], v[6:7], v[130:131] op_sel_hi:[1,0]\n"
    "v_mfma_f32_16x16x32_bf16 v[60:63], a[124:127], v[212:215], v[60:63]\n"
    "v_cvt_pk_bf16_f32 v4, v4, v5\n"
    "v_cvt_pk_bf16_f32 v5, v6, v7\n"
    "ds_write_b64 v245, v[4:5]\n"
    "v_mfma_f32_16x16x32_bf16 v[132:135], a[128:131], v[212:215], v[132:135]\n"
    "v_pk_mul_f32 v[8:9], v[8:9], v[130:131] op_sel_hi:[1,0]\n"
    "v_pk_mul_f32 v[10:11], v[10:11], v[130:131] op_sel_hi:[1,0]\n"
    "v_cvt_pk_bf16_f32 v8, v8, v9\n"
    "v_mfma_f32_16x16x32_bf16 v[64:67], a[96:99], v[216:219], v[64:67]\n"
    "v_cvt_pk_bf16_f32 v9, v10, v11\n"
    "ds_write_b64 v246, v[8:9]\n"
    "v_pk_mul_f32 v[12:13], v[12:13], v[130:131] op_sel_hi:[1,0]\n"
    "v_mfma_f32_16x16x32_bf16 v[68:71], a[100:103], v[216:219], v[68:71]\n"
    "v_pk_mul_f32 v[14:15], v[14:15], v[130:131] op_sel_hi:[1,0]\n"
    "v_cvt_pk_bf16_f32 v12, v12, v13\n"
    "v_cvt_pk_bf16_f32 v13, v14, v15\n"
    "v_mfma_f32_16x16x32_bf16 v[72:75], a[104:107], v[216:219], v[72:75]\n"
    "ds_write_b64 v247, v[12:13]\n"
    "v_pk_mul_f32 v[16:17], v[16:17], v[130:131] op_sel_hi:[1,0]\n"
    "v_pk_mul_f32 v[18:19], v[18:19], v[130:131] op_sel_hi:[1,0]\n"
    "v_mfma_f32_16x16x32_bf16 v[76:79], a[108:111], v[216:219], v[76:79]\n"
    "v_cvt_pk_bf16_f32 v16, v16, v17\n"
    "v_cvt_pk_bf16_f32 v17, v18, v19\n"
    "ds_write_b64 v244, v[16:17] offset:128\n"
    "v_mfma_f32_16x16x32_bf16 v[80:83], a[112:115], v[216:219], v[80:83]\n"
    "v_pk_mul_f32 v[20:21], v[20:21], v[130:131] op_sel_hi:[1,0]\n"
    "v_pk_mul_f32 v[22:23], v[22:23], v[130:131] op_sel_hi:[1,0]\n"
    "v_cvt_pk_bf16_f32 v20, v20, v21\n"
    "v_mfma_f32_16x16x32_bf16 v[84:87], a[116:119], v[216:219], v[84:87]\n"
    "v_cvt_pk_bf16_f32 v21, v22, v23\n"
    "ds_write_b64 v245, v[20:21] offset:128\n"
    "v_pk_mul_f32 v[24:25], v[24:25], v[130:131] op_sel_hi:[1,0]\n"
    "v_mfma_f32_16x16x32_bf16 v[88:91], a[120:123], v[216:219], v[88:91]\n"
    "v_pk_mul_f32 v[26:27], v[26:27], v[130:131] op_sel_hi:[1,0]\n"
    "v_cvt_pk_bf16_f32 v24, v24, v25\n"
    "v_cvt_pk_bf16_f32 v25, v26, v27\n"
    "v_mfma_f32_16x16x32_bf16 v[92:95], a[124:127], v[216:219], v[92:95]\n"
    "ds_write_b64 v246, v[24:25] offset:128\n"
    "v_pk_mul_f32 v[28:29], v[28:29], v[130:131] op_sel_hi:[1,0]\n"
    "v_pk_mul_f32 v[30:31], v[30:31], v[130:131] op_sel_hi:[1,0]\n"
    "v_mfma_f32_16x16x32_bf16 v[136:139], a[128:131], v[216:219], v[136:139]\n"
    "v_cvt_pk_bf16_f32 v28, v28, v29\n"
    "v_cvt_pk_bf16_f32 v29, v30, v31\n"
    "ds_write_b64 v247, v[28:29] offset:128\n"
    "v_mfma_f32_16x16x32_bf16 v[96:99], a[96:99], v[220:223], v[96:99]\n"
    "s_waitcnt lgkmcnt(0)\n"
    "ds_read_b128 v[160:163], v248\n"
    "ds_read_b128 v[164:167], v249\n"
    "v_mfma_f32_16x16x32_bf16 v[100:103], a[100:103], v[220:223], v[100:103]\n"
    "ds_read_b128 v[168:171], v248 offset:2048\n"
    "ds_read_b128 v[172:175], v249 offset:2048\n"
    "s_waitcnt lgkmcnt(3)\n"
    "v_mfma_f32_16x16x32_bf16 v[104:107], a[104:107], v[220:223], v[104:107]\n"
    "buffer_store_dwordx4 v[160:163], v250, s[52:55], s59 offen sc1 nt\n"
    "s_waitcnt lgkmcnt(2)\n"
    "buffer_store_dwordx4 v[164:167], v250, s[52:55], s59 offen offset:1024 sc1 nt\n"
    "v_mfma_f32_16x16x32_bf16 v[108:111], a[108:111], v[220:223], v[108:111]\n"
    "s_waitcnt lgkmcnt(1)\n"
    "buffer_store_dwordx4 v[168:171], v250, s[52:55], s59 offen offset:2048 sc1 nt\n"
    "s_waitcnt lgkmcnt(0)\n"
    "v_mfma_f32_16x16x32_bf16 v[112:115], a[112:115], v[220:223], v[112:115]\n"
    "buffer_store_dwordx4 v[172:175], v250, s[52:55], s59 offen offset:3072 sc1 nt\n"
    "s_add_u32 s59, s59, 4096\n"
    "s_nop 3\n"
    "v_mfma_f32_16x16x32_bf16 v[116:119], a[116:119], v[220:223], v[116:119]\n"
    "v_cmp_ge_f32 vcc, s62, v132\n"
    "v_rcp_f32 v134, v132\n"
    "s_nop 1\n"
    "v_mfma_f32_16x16x32_bf16 v[120:123], a[120:123], v[220:223], v[120:123]\n"
    "s_and_b64 s[66:67], s[66:67], vcc\n"
    "v_pk_mul_f32 v[32:33], v[32:33], v[134:135] op_sel_hi:[1,0]\n"
    "v_pk_mul_f32 v[34:35], v[34:35], v[134:135] op_sel_hi:[1,0]\n"
    "v_mfma_f32_16x16x32_bf16 v[124:127], a[124:127], v[220:223], v[124:127]\n"
    "v_cvt_pk_bf16_f32 v32, v32, v33\n"
    "v_cvt_pk_bf16_f32 v33, v34, v35\n"
    "ds_write_b64 v244, v[32:33] offset:4096\n"
    "v_mfma_f32_16x16x32_bf16 v[140:143], a[128:131], v[220:223], v[140:143]\n"
    "v_pk_mul_f32 v[36:37], v[36:37], v[134:135] op_sel_hi:[1,0]\n"
    "v_pk_mul_f32 v[38:39], v[38:39], v[134:135] op_sel_hi:[1,0]\n"
    "v_cvt_pk_bf16_f32 v36, v36, v37\n"
    "s_nop 7\n"
    "s_nop 1\n"
    "v_cvt_pk_bf16_f32 v37, v38, v39\n"
    "ds_write_b64 v245, v[36:37] offset:4096\n"
    "v_pk_mul_f32 v[40:41], v[40:41], v[134:135] op_sel_hi:[1,0]\n"
    "v_pk_mul_f32 v[42:43], v[42:43], v[134:135] op_sel_hi:[1,0]\n"
    "v_cvt_pk_bf16_f32 v40, v40, v41\n"
    "v_cvt_pk_bf16_f32 v41, v42, v43\n"
    "ds_write_b64 v246, v[40:41] offset:4096\n"
    "v_pk_mul_f32 v[44:45], v[44:45], v[134:135] op_sel_hi:[1,0]\n"
    "v_pk_mul_f32 v[46:47], v[46:47], v[134:135] op_sel_hi:[1,0]\n"
    "v_cvt_pk_bf16_f32 v44, v44, v45\n"
    "v_cvt_pk_bf16_f32 v45, v46, v47\n"
    "ds_write_b64 v247, v[44:45] offset:4096\n"
    "v_pk_mul_f32 v[48:49], v[48:49], v[134:135] op_sel_hi:[1,0]\n"
    "v_pk_mul_f32 v[50:51], v[50:51], v[134:135] op_sel_hi:[1,0]\n"
    "v_cvt_pk_bf16_f32 v48, v48, v49\n"
    "v_cvt_pk_bf16_f32 v49, v50, v51\n"
    "ds_write_b64 v244, v[48:49] offset:4224\n"
    "v_pk_mul_f32 v[52:53], v[52:53], v[134:135] op_sel_hi:[1,0]\n"
    "v_pk_mul_f32 v[54:55], v[54:55], v[134:135] op_sel_hi:[1,0]\n"
    "v_cvt_pk_bf16_f32 v52, v52, v53\n"
    "v_cvt_pk_bf16_f32 v53, v54, v55\n"
    "ds_write_b64 v245, v[52:53] offset:4224\n"
    "v_pk_mul_f32 v[56:57], v[56:57], v[134:135] op_sel_hi:[1,0]\n"
    "v_pk_mul_f32 v[58:59], v[58:59], v[134:135] op_sel_hi:[1,0]\n"
    "v_cvt_pk_bf16_f32 v56, v56, v57\n"
    "v_cvt_pk_bf16_f32 v57, v58, v59\n"
    "ds_write_b64 v246, v[56:57] offset:4224\n"
    "v_pk_mul_f32 v[60:61], v[60:61], v[134:135] op_sel_hi:[1,0]\n"
    "v_pk_mul_f32 v[62:63], v[62:63], v[134:135] op_sel_hi:[1,0]\n"
    "v_cvt_pk_bf16_f32 v60, v60, v61\n"
    "v_cvt_pk_bf16_f32 v61, v62, v63\n"
    "ds_write_b64 v247, v[60:61] offset:4224\n"
    "s_waitcnt lgkmcnt(0)\n"
    "ds_read_b128 v[192:195], v248 offset:4096\n"
    "ds_read_b128 v[196:199], v249 offset:4096\n"
    "ds_read_b128 v[200:203], v248 offset:6144\n"
    "ds_read_b128 v[204:207], v249 offset:6144\n"
    "s_waitcnt lgkmcnt(3)\n"
    "buffer_store_dwordx4 v[192:195], v250, s[52:55], s59 offen sc1 nt\n"
    "s_waitcnt lgkmcnt(2)\n"
    "buffer_store_dwordx4 v[196:199], v250, s[52:55], s59 offen offset:1024 sc1 nt\n"
    "s_waitcnt lgkmcnt(1)\n"
    "buffer_store_dwordx4 v[200:203], v250, s[52:55], s59 offen offset:2048 sc1 nt\n"
    "s_waitcnt lgkmcnt(0)\n"
    "buffer_store_dwordx4 v[204:207], v250, s[52:55], s59 offen offset:3072 sc1 nt\n"
    "s_add_u32 s59, s59, 4096\n"
    "s_nop 3\n"
    "v_cmp_ge_f32 vcc, s62, v136\n"
    "v_rcp_f32 v138, v136\n"
    "s_nop 1\n"
    "s_and_b64 s[66:67], s[66:67], vcc\n"
    "v_pk_mul_f32 v[64:65], v[64:65], v[138:139] op_sel_hi:[1,0]\n"
    "v_pk_mul_f32 v[66:67], v[66:67], v[138:139] op_sel_hi:[1,0]\n"
    "v_cvt_pk_bf16_f32 v64, v64, v65\n"
    "v_cvt_pk_bf16_f32 v65, v66, v67\n"
    "ds_write_b64 v244, v[64:65] offset:8192\n"
    "v_pk_mul_f32 v[68:69], v[68:69], v[138:139] op_sel_hi:[1,0]\n"
    "v_pk_mul_f32 v[70:71], v[70:71], v[138:139] op_sel_hi:[1,0]\n"
    "v_cvt_pk_bf16_f32 v68, v68, v69\n"
    "v_cvt_pk_bf16_f32 v69, v70, v71\n"
    "ds_write_b64 v245, v[68:69] offset:8192\n"
    "v_pk_mul_f32 v[72:73], v[72:73], v[138:139] op_sel_hi:[1,0]\n"
    "v_pk_mul_f32 v[74:75], v[74:75], v[138:139] op_sel_hi:[1,0]\n"
    "v_cvt_pk_bf16_f32 v72, v72, v73\n"
    "v_cvt_pk_bf16_f32 v73, v74, v75\n"
    "ds_write_b64 v246, v[72:73] offset:8192\n"
    "v_pk_mul_f32 v[76:77], v[76:77], v[138:139] op_sel_hi:[1,0]\n"
    "v_pk_mul_f32 v[78:79], v[78:79], v[138:139] op_sel_hi:[1,0]\n"
    "v_cvt_pk_bf16_f32 v76, v76, v77\n"
    "v_cvt_pk_bf16_f32 v77, v78, v79\n"
    "ds_write_b64 v247, v[76:77] offset:8192\n"
    "v_pk_mul_f32 v[80:81], v[80:81], v[138:139] op_sel_hi:[1,0]\n"
    "v_pk_mul_f32 v[82:83], v[82:83], v[138:139] op_sel_hi:[1,0]\n"
    "v_cvt_pk_bf16_f32 v80, v80, v81\n"
    "v_cvt_pk_bf16_f32 v81, v82, v83\n"
    "ds_write_b64 v244, v[80:81] offset:8320\n"
    "v_pk_mul_f32 v[84:85], v[84:85], v[138:139] op_sel_hi:[1,0]\n"
    "v_pk_mul_f32 v[86:87], v[86:87], v[138:139] op_sel_hi:[1,0]\n"
    "v_cvt_pk_bf16_f32 v84, v84, v85\n"
    "v_cvt_pk_bf16_f32 v85, v86, v87\n"
    "ds_write_b64 v245, v[84:85] offset:8320\n"
    "v_pk_mul_f32 v[88:89], v[88:89], v[138:139] op_sel_hi:[1,0]\n"
    "v_pk_mul_f32 v[90:91], v[90:91], v[138:139] op_sel_hi:[1,0]\n"
    "v_cvt_pk_bf16_f32 v88, v88, v89\n"
    "v_cvt_pk_bf16_f32 v89, v90, v91\n"
    "ds_write_b64 v246, v[88:89] offset:8320\n"
    "v_pk_mul_f32 v[92:93], v[92:93], v[138:139] op_sel_hi:[1,0]\n"
    "v_pk_mul_f32 v[94:95], v[94:95], v[138:139] op_sel_hi:[1,0]\n"
    "v_cvt_pk_bf16_f32 v92, v92, v93\n"
    "v_cvt_pk_bf16_f32 v93, v94, v95\n"
    "ds_write_b64 v247, v[92:93] offset:8320\n"
    "s_waitcnt lgkmcnt(0)\n"
    "ds_read_b128 v[160:163], v248 offset:8192\n"
    "ds_read_b128 v[164:167], v249 offset:8192\n"
    "ds_read_b128 v[168:171], v248 offset:10240\n"
    "ds_read_b128 v[172:175], v249 offset:10240\n"
    "s_waitcnt lgkmcnt(3)\n"
    "buffer_store_dwordx4 v[160:163], v250, s[52:55], s59 offen sc1 nt\n"
    "s_waitcnt lgkmcnt(2)\n"
    "buffer_store_dwordx4 v[164:167], v250, s[52:55], s59 offen offset:1024 sc1 nt\n"
    "s_waitcnt lgkmcnt(1)\n"
    "buffer_store_dwordx4 v[168:171], v250, s[52:55], s59 offen offset:2048 sc1 nt\n"
    "s_waitcnt lgkmcnt(0)\n"
    "buffer_store_dwordx4 v[172:175], v250, s[52:55], s59 offen offset:3072 sc1 nt\n"
    "s_add_u32 s59, s59, 4096\n"
    "s_nop 3\n"
    "v_cmp_ge_f32 vcc, s62, v140\n"
    "v_rcp_f32 v142, v140\n"
    "s_nop 1\n"
    "s_and_b64 s[66:67], s[66:67], vcc\n"
    "v_pk_mul_f32 v[96:97], v[96:97], v[142:143] op_sel_hi:[1,0]\n"
    "v_pk_mul_f32 v[98:99], v[98:99], v[142:143] op_sel_hi:[1,0]\n"
    "v_cvt_pk_bf16_f32 v96, v96, v97\n"
    "v_cvt_pk_bf16_f32 v97, v98, v99\n"
    "ds_write_b64 v244, v[96:97] offset:12288\n"
    "v_pk_mul_f32 v[100:101], v[100:101], v[142:143] op_sel_hi:[1,0]\n"
    "v_pk_mul_f32 v[102:103], v[102:103], v[142:143] op_sel_hi:[1,0]\n"
    "v_cvt_pk_bf16_f32 v100, v100, v101\n"
    "v_cvt_pk_bf16_f32 v101, v102, v103\n"
    "ds_write_b64 v245, v[100:101] offset:12288\n"
    "v_pk_mul_f32 v[104:105], v[104:105], v[142:143] op_sel_hi:[1,0]\n"
    "v_pk_mul_f32 v[106:107], v[106:107], v[142:143] op_sel_hi:[1,0]\n"
    "v_cvt_pk_bf16_f32 v104, v104, v105\n"
    "v_cvt_pk_bf16_f32 v105, v106, v107\n"
    "ds_write_b64 v246, v[104:105] offset:12288\n"
    "v_pk_mul_f32 v[108:109], v[108:109], v[142:143] op_sel_hi:[1,0]\n"
    "v_pk_mul_f32 v[110:111], v[110:111], v[142:143] op_sel_hi:[1,0]\n"
    "v_cvt_pk_bf16_f32 v108, v108, v109\n"
    "v_cvt_pk_bf16_f32 v109, v110, v111\n"
    "ds_write_b64 v247, v[108:109] offset:12288\n"
    "v_pk_mul_f32 v[112:113], v[112:113], v[142:143] op_sel_hi:[1,0]\n"
    "v_pk_mul_f32 v[114:115], v[114:115], v[142:143] op_sel_hi:[1,0]\n"
    "v_cvt_pk_bf16_f32 v112, v112, v113\n"
    "v_cvt_pk_bf16_f32 v113, v114, v115\n"
    "ds_write_b64 v244, v[112:113] offset:12416\n"
    "v_pk_mul_f32 v[116:117], v[116:117], v[142:143] op_sel_hi:[1,0]\n"
    "v_pk_mul_f32 v[118:119], v[118:119], v[142:143] op_sel_hi:[1,0]\n"
    "v_cvt_pk_bf16_f32 v116, v116, v117\n"
    "v_cvt_pk_bf16_f32 v117, v118, v119\n"
    "ds_write_b64 v245, v[116:117] offset:12416\n"
    "v_pk_mul_f32 v[120:121], v[120:121], v[142:143] op_sel_hi:[1,0]\n"
    "v_pk_mul_f32 v[122:123], v[122:123], v[142:143] op_sel_hi:[1,0]\n"
    "v_cvt_pk_bf16_f32 v120, v120, v121\n"
    "v_cvt_pk_bf16_f32 v121, v122, v123\n"
    "ds_write_b64 v246, v[120:121] offset:12416\n"
    "v_pk_mul_f32 v[124:125], v[124:125], v[142:143] op_sel_hi:[1,0]\n"
    "v_pk_mul_f32 v[126:127], v[126:127], v[142:143] op_sel_hi:[1,0]\n"
    "v_cvt_pk_bf16_f32 v124, v124, v125\n"
    "v_cvt_pk_bf16_f32 v125, v126, v127\n"
    "ds_write_b64 v247, v[124:125] offset:12416\n"
    "s_waitcnt lgkmcnt(0)\n"
    "ds_read_b128 v[192:195], v248 offset:12288\n"
    "ds_read_b128 v[196:199], v249 offset:12288\n"
    "ds_read_b128 v[200:203], v248 offset:14336\n"
    "ds_read_b128 v[204:207], v249 offset:14336\n"
    "s_waitcnt lgkmcnt(3)\n"
    "buffer_store_dwordx4 v[192:195], v250, s[52:55], s59 offen sc1 nt\n"
    "s_waitcnt lgkmcnt(2)\n"
    "buffer_store_dwordx4 v[196:199], v250, s[52:55], s59 offen offset:1024 sc1 nt\n"
    "s_waitcnt lgkmcnt(1)\n"
    "buffer_store_dwordx4 v[200:203], v250, s[52:55], s59 offen offset:2048 sc1 nt\n"
    "s_waitcnt lgkmcnt(0)\n"
    "buffer_store_dwordx4 v[204:207], v250, s[52:55], s59 offen offset:3072 sc1 nt\n"
    "s_nop 3\n"
    "s_cmp_eq_u64 s[66:67], -1\n"
    "s_cselect_b32 %[flag], 0, 1\n"
    "s_waitcnt vmcnt(0)\n"
    "s_branch Lfin_%=\n"
    "Lb0_1_%=:\n"
    "s_waitcnt vmcnt(14)\n"
    "s_barrier\n"
    "s_branch Lb0_1_%=b\n"
    "Lpw2_%=:\n"
    "s_memrealtime s[76:77]\n"
    "s_waitcnt lgkmcnt(0)\n"
    "s_sub_u32 s79, s78, s76\n"
    "s_cmp_gt_i32 s79, 0\n"
    "s_cbranch_scc1 Lpw2_%=\n"
    "s_branch Lpw2_%=b\n"
    "Lpw3_%=:\n"
    "s_memrealtime s[76:77]\n"
    "s_waitcnt lgkmcnt(0)\n"
    "s_sub_u32 s79, s78, s76\n"
    "s_cmp_gt_i32 s79, 0\n"
    "s_cbranch_scc1 Lpw3_%=\n"
    "s_branch Lpw3_%=b\n"
    "Lpw4_%=:\n"
    "s_memrealtime s[76:77]\n"
    "s_waitcnt lgkmcnt(0)\n"
    "s_sub_u32 s79, s78, s76\n"
    "s_cmp_gt_i32 s79, 0\n"
    "s_cbranch_scc1 Lpw4_%=\n"
    "s_branch Lpw4_%=b\n"
    "Lpw5_%=:\n"
    "s_memrealtime s[76:77]\n"
    "s_waitcnt lgkmcnt(0)\n"
    "s_sub_u32 s79, s78, s76\n"
    "s_cmp_gt_i32 s79, 0\n"
    "s_cbranch_scc1 Lpw5_%=\n"
    "s_branch Lpw5_%=b\n"
    "Lpw6_%=:\n"
    "s_memrealtime s[76:77]\n"
    "s_waitcnt lgkmcnt(0)\n"
    "s_sub_u32 s79, s78, s76\n"
    "s_cmp_gt_i32 s79, 0\n"
    "s_cbranch_scc1 Lpw6_%=\n"
    "s_branch Lpw6_%=b\n"
    "Lpw7_%=:\n"
    "s_memrealtime s[76:77]\n"
    "s_waitcnt lgkmcnt(0)\n"
    "s_sub_u32 s79, s78, s76\n"
    "s_cmp_gt_i32 s79, 0\n"
    "s_cbranch_scc1 Lpw7_%=\n"
    "s_branch Lpw7_%=b\n"
    "Lpw8_%=:\n"
    "s_memrealtime s[76:77]\n"
    "s_waitcnt lgkmcnt(0)\n"
    "s_sub_u32 s79, s78, s76\n"
    "s_cmp_gt_i32 s79, 0\n"
    "s_cbranch_scc1 Lpw8_%=\n"
    "s_branch Lpw8_%=b\n"
    "Lpw9_%=:\n"
    "s_memrealtime s[76:77]\n"
    "s_waitcnt lgkmcnt(0)\n"
    "s_sub_u32 s79, s78, s76\n"
    "s_cmp_gt_i32 s79, 0\n"
    "s_cbranch_scc1 Lpw9_%=\n"
    "s_branch Lpw9_%=b\n"
    "Lpw10_%=:\n"
    "s_memrealtime s[76:77]\n"
    "s_waitcnt lgkmcnt(0)\n"
    "s_sub_u32 s79, s78, s76\n"
    "s_cmp_gt_i32 s79, 0\n"
    "s_cbranch_scc1 Lpw10_%=\n"
    "s_branch Lpw10_%=b\n"
    "Lpw11_%=:\n"
    "s_memrealtime s[76:77]\n"
    "s_waitcnt lgkmcnt(0)\n"
    "s_sub_u32 s79, s78, s76\n"
    "s_cmp_gt_i32 s79, 0\n"
    "s_cbranch_scc1 Lpw11_%=\n"
    "s_branch Lpw11_%=b\n"
    "Lpw12_%=:\n"
    "s_memrealtime s[76:77]\n"
    "s_waitcnt lgkmcnt(0)\n"
    "s_sub_u32 s79, s78, s76\n"
    "s_cmp_gt_i32 s79, 0\n"
    "s_cbranch_scc1 Lpw12_%=\n"
    "s_branch Lpw12_%=b\n"
    "Lpw13_%=:\n"
    "s_memrealtime s[76:77]\n"
    "s_waitcnt lgkmcnt(0)\n"
    "s_sub_u32 s79, s78, s76\n"
    "s_cmp_gt_i32 s79, 0\n"
    "s_cbranch_scc1 Lpw13_%=\n"
    "s_branch Lpw13_%=b\n"
    "Lpw14_%=:\n"
    "s_memrealtime s[76:77]\n"
    "s_waitcnt lgkmcnt(0)\n"
    "s_sub_u32 s79, s78, s76\n"
    "s_cmp_gt_i32 s79, 0\n"
    "s_cbranch_scc1 Lpw14_%=\n"
    "s_branch Lpw14_%=b\n"
    "Lpw15_%=:\n"
    "s_memrealtime s[76:77]\n"
    "s_waitcnt lgkmcnt(0)\n"
    "s_sub_u32 s79, s78, s76\n"
    "s_cmp_gt_i32 s79, 0\n"
    "s_cbranch_scc1 Lpw15_%=\n"
    "s_branch Lpw15_%=b\n"
    "Lpw16_%=:\n"
    "s_memrealtime s[76:77]\n"
    "s_waitcnt lgkmcnt(0)\n"
    "s_sub_u32 s79, s78, s76\n"
    "s_cmp_gt_i32 s79, 0\n"
    "s_cbranch_scc1 Lpw16_%=\n"
    "s_branch Lpw16_%=b\n"
    "Lpw17_%=:\n"
    "s_memrealtime s[76:77]\n"
    "s_waitcnt lgkmcnt(0)\n"
    "s_sub_u32 s79, s78, s76\n"
    "s_cmp_gt_i32 s79, 0\n"
    "s_cbranch_scc1 Lpw17_%=\n"
    "s_branch Lpw17_%=b\n"
    "Lb0_18_%=:\n"
    "s_waitcnt vmcnt(14)\n"
    "s_barrier\n"
    "s_branch Lb0_18_%=b\n"
    "Lpw19_%=:\n"
    "s_memrealtime s[76:77]\n"
    "s_waitcnt lgkmcnt(0)\n"
    "s_sub_u32 s79, s78, s76\n"
    "s_cmp_gt_i32 s79, 0\n"
    "s_cbranch_scc1 Lpw19_%=\n"
    "s_branch Lpw19_%=b\n"
    "Lpw20_%=:\n"
    "s_memrealtime s[76:77]\n"
    "s_waitcnt lgkmcnt(0)\n"
    "s_sub_u32 s79, s78, s76\n"
    "s_cmp_gt_i32 s79, 0\n"
    "s_cbranch_scc1 Lpw20_%=\n"
    "s_branch Lpw20_%=b\n"
    "Lpw21_%=:\n"
    "s_memrealtime s[76:77]\n"
    "s_waitcnt lgkmcnt(0)\n"
    "s_sub_u32 s79, s78, s76\n"
    "s_cmp_gt_i32 s79, 0\n"
    "s_cbranch_scc1 Lpw21_%=\n"
    "s_branch Lpw21_%=b\n"
    "Lpw22_%=:\n"
    "s_memrealtime s[76:77]\n"
    "s_waitcnt lgkmcnt(0)\n"
    "s_sub_u32 s79, s78, s76\n"
    "s_cmp_gt_i32 s79, 0\n"
    "s_cbranch_scc1 Lpw22_%=\n"
    "s_branch Lpw22_%=b\n"
    "Lpw23_%=:\n"
    "s_memrealtime s[76:77]\n"
    "s_waitcnt lgkmcnt(0)\n"
    "s_sub_u32 s79, s78, s76\n"
    "s_cmp_gt_i32 s79, 0\n"
    "s_cbranch_scc1 Lpw23_%=\n"
    "s_branch Lpw23_%=b\n"
    "Lpw24_%=:\n"
    "s_memrealtime s[76:77]\n"
    "s_waitcnt lgkmcnt(0)\n"
    "s_sub_u32 s79, s78, s76\n"
    "s_cmp_gt_i32 s79, 0\n"
    "s_cbranch_scc1 Lpw24_%=\n"
    "s_branch Lpw24_%=b\n"
    "Lpw25_%=:\n"
    "s_memrealtime s[76:77]\n"
    "s_waitcnt lgkmcnt(0)\n"
    "s_sub_u32 s79, s78, s76\n"
    "s_cmp_gt_i32 s79, 0\n"
    "s_cbranch_scc1 Lpw25_%=\n"
    "s_branch Lpw25_%=b\n"
    "Lpw26_%=:\n"
    "s_memrealtime s[76:77]\n"
    "s_waitcnt lgkmcnt(0)\n"
    "s_sub_u32 s79, s78, s76\n"
    "s_cmp_gt_i32 s79, 0\n"
    "s_cbranch_scc1 Lpw26_%=\n"
    "s_branch Lpw26_%=b\n"
    "Lfin_%=:\n"
    : [flag] "=&s"(flag)
    : [q] "s"(qp), [k] "s"(kp), [v] "s"(vp), [o] "s"(op), [w] "s"(w), [lds] "s"(ldsb), [bh] "s"(bh)
    : "v0", "v1", "v2", "v3", "v4", "v5", "v6", "v7", "v8", "v9", "v10", "v11", "v12", "v13", "v14", "v15", "v16", "v17", "v18", "v19", "v20", "v21", "v22", "v23", "v24", "v25", "v26", "v27", "v28", "v29", "v30", "v31", "v32", "v33", "v34", "v35", "v36", "v37", "v38", "v39", "v40", "v41", "v42", "v43", "v44", "v45", "v46", "v47", "v48", "v49", "v50", "v51", "v52", "v53", "v54", "v55", "v56", "v57", "v58", "v59", "v60", "v61", "v62", "v63", "v64", "v65", "v66", "v67", "v68", "v69", "v70", "v71", "v72", "v73", "v74", "v75", "v76", "v77", "v78", "v79", "v80", "v81", "v82", "v83", "v84", "v85", "v86", "v87", "v88", "v89", "v90", "v91", "v92", "v93", "v94", "v95", "v96", "v97", "v98", "v99", "v100", "v101", "v102", "v103", "v104", "v105", "v106", "v107", "v108", "v109", "v110", "v111", "v112", "v113", "v114", "v115", "v116", "v117", "v118", "v119", "v120", "v121", "v122", "v123", "v124", "v125", "v126", "v127", "v128", "v129", "v130", "v131", "v132", "v133", "v134", "v135", "v136", "v137", "v138", "v139", "v140", "v141", "v142", "v143", "v144", "v145", "v146", "v147", "v148", "v149", "v150", "v151", "v152", "v153", "v154", "v155", "v156", "v157", "v158", "v159", "v160", "v161", "v162", "v163", "v164", "v165", "v166", "v167", "v168", "v169", "v170", "v171", "v172", "v173", "v174", "v175", "v176", "v177", "v178", "v179", "v180", "v181", "v182", "v183", "v184", "v185", "v186", "v187", "v188", "v189", "v190", "v191", "v192", "v193", "v194", "v195", "v196", "v197", "v198", "v199", "v200", "v201", "v202", "v203", "v204", "v205", "v206", "v207", "v208", "v209", "v210", "v211", "v212", "v213", "v214", "v215", "v216", "v217", "v218", "v219", "v220", "v221", "v222", "v223", "v224", "v225", "v226", "v227", "v228", "v229", "v230", "v231", "v232", "v233", "v234", "v235", "v236", "v237", "v238", "v239", "v240", "v241", "v242", "v243", "v244", "v245", "v246", "v247", "v248", "v249", "v250", "v251", "v252", "v253", "a0", "a1", "a2", "a3", "a4", "a5", "a6", "a7", "a8", "a9", "a10", "a11", "a12", "a13", "a14", "a15", "a16", "a17", "a18", "a19", "a20", "a21", "a22", "a23", "a24", "a25", "a26", "a27", "a28", "a29", "a30", "a31", "a32", "a33", "a34", "a35", "a36", "a37", "a38", "a39", "a40", "a41", "a42", "a43", "a44", "a45", "a46", "a47", "a48", "a49", "a50", "a51", "a52", "a53", "a54", "a55", "a56", "a57", "a58", "a59", "a60", "a61", "a62", "a63", "a64", "a65", "a66", "a67", "a68", "a69", "a70", "a71", "a72", "a73", "a74", "a75", "a76", "a77", "a78", "a79", "a80", "a81", "a82", "a83", "a84", "a85", "a86", "a87", "a88", "a89", "a90", "a91", "a92", "a93", "a94", "a95", "a96", "a97", "a98", "a99", "a100", "a101", "a102", "a103", "a104", "a105", "a106", "a107", "a108", "a109", "a110", "a111", "a112", "a113", "a114", "a115", "a116", "a117", "a118", "a119", "a120", "a121", "a122", "a123", "a124", "a125", "a126", "a127", "a128", "a129", "a130", "a131", "s40", "s41", "s42", "s43", "s44", "s45", "s46", "s47", "s48", "s49", "s50", "s51", "s52", "s53", "s54", "s55", "s56", "s57", "s58", "s59", "s60", "s61", "s62", "s63", "s64", "s65", "s66", "s67", "s68", "s69", "s70", "s71", "s72", "s73", "s74", "s75", "s76", "s77", "s78", "s79", "s80", "s81", "s82", "s83", "s84", "s85", "s86", "s87", "s88", "s89", "s90", "s91", "s92", "s93", "s94", "s95", "vcc", "scc", "memory");
  if (flag) {
    asm volatile("s_waitcnt vmcnt(0)" ::: "memory");
    exact_rows(qp, kp, vp, op, w);
  }
}

extern "C" void kernel_launch(void* const* d_in, const int* in_sizes, int n_in, void* d_out, int out_size, void* d_ws, size_t ws_size, hipStream_t stream) {
  (void)in_sizes; (void)n_in; (void)out_size; (void)ws_size;
  (void)hipFuncSetAttribute((const void*)standalone__gqa_softmax__near_ridge_kernel, hipFuncAttributeMaxDynamicSharedMemorySize, 163840);
  (void)d_ws;
  hipLaunchKernelGGL(standalone__gqa_softmax__near_ridge_kernel, dim3(256), dim3(256), 163840, stream, (const char*)d_in[0], (const char*)d_in[1], (const char*)d_in[2], (char*)d_out);
}
```
